```python
import torch

def _softmax(x, axis=-1):
    return torch.softmax(x, dim=axis)
def _gelu(x):
    return torch.nn.functional.gelu(x, approximate="tanh")
def _silu(x):
    return torch.nn.functional.silu(x)
def _sigmoid(x):
    return torch.sigmoid(x)
def _layernorm(x, axis=-1, eps=1e-5):
    m = x.mean(dim=axis, keepdim=True)
    v = x.var(dim=axis, keepdim=True, unbiased=False)
    return (x - m) / torch.sqrt(v + eps)
def _scatter_add(tgt, idx, src, axis=0):
    return tgt.clone().index_add_(axis, idx, src)
def _segment_sum(src, seg, n):
    out = torch.zeros((n,) + src.shape[1:], dtype=src.dtype)
    return out.index_add_(0, seg, src)
def _pad(x, pw):
    flat = tuple(v for pair in reversed(pw) for v in pair)
    return torch.nn.functional.pad(x, flat)

kernel_name = 'parallel_sum__sliding_block_cached__dense_activation_silu__decode'

def setup_inputs():

    _w = (lambda a: (lambda t: t.to(torch.bfloat16) if t.is_floating_point() else t)(torch.as_tensor(a)))
    def _A_setup():
        B, Sq, D = 8, 1, 512
        Hq, Hkv, Sk, Dh = 32, 8, 4096, 128
        assert Hq % Hkv == 0, (Hq, Hkv)
        return {
            "x": _w((lambda *s: torch.randn(*s, dtype=torch.bfloat16) * 0.05)(B, Sq, D)),
            "k_cache": _w((lambda *s: torch.randn(*s, dtype=torch.bfloat16) * 3.2)(B, Hkv, Sk, Dh)),
            "v_cache": _w((lambda *s: torch.randn(*s, dtype=torch.bfloat16) * 0.8)(B, Hkv, Sk, Dh)),
            "wq": _w((lambda *s: torch.randn(*s, dtype=torch.bfloat16) * 3.2)(D, Hq * Dh) * (D ** -0.5)),
            "wo": _w((lambda *s: torch.randn(*s, dtype=torch.bfloat16) * 0.8)(Hq * Dh, D) * ((Hq * Dh) ** -0.5)),
        }
    _A_in = _A_setup()

    def _B_setup():
        B, S, D, F = 8, 1, 512, 1792
        return {
            "x": _w((lambda *s: torch.randn(*s, dtype=torch.bfloat16) * 0.05)(B, S, D)),
            "w1": _w((lambda *s: torch.randn(*s, dtype=torch.bfloat16) * 3.2)(D, F) * (D ** -0.5)),
            "w2": _w((lambda *s: torch.randn(*s, dtype=torch.bfloat16) * 0.4)(F, D) * (F ** -0.5)),
        }
    _B_in = _B_setup()

    return {
        "A_x": _A_in["x"],
        "A_k_cache": _A_in["k_cache"],
        "A_v_cache": _A_in["v_cache"],
        "A_wq": _A_in["wq"],
        "A_wo": _A_in["wo"],
        "B_x": _B_in["x"],
        "B_w1": _B_in["w1"],
        "B_w2": _B_in["w2"],
    }

def reference(A_x, A_k_cache, A_v_cache, A_wq, A_wo, B_x, B_w1, B_w2):
    def _A_ref(x, k_cache, v_cache, wq, wo):
        B, Sq, D = x.shape
        Hq, Dh = 32, 128
        Hkv, Sk = k_cache.shape[1], k_cache.shape[2]
        W = min(4096, max(256, Sk // 4))
        rep = Hq // Hkv
        q = (lambda x, *d: x.permute(*d))((x @ wq).reshape(B, Sq, Hq, Dh), 0, 2, 1, 3)
        k = (lambda x, n, axis: torch.repeat_interleave(x, n, dim=axis))(k_cache[:, :, Sk - W:, :], rep, axis=1)
        v = (lambda x, n, axis: torch.repeat_interleave(x, n, dim=axis))(v_cache[:, :, Sk - W:, :], rep, axis=1)
        scores = torch.einsum("bhqd,bhkd->bhqk", q, k) * (Dh ** -0.5)
        p = _softmax(scores, axis=-1)
        o = torch.einsum("bhqk,bhkd->bhqd", p, v)
        return (lambda x, *d: x.permute(*d))(o, 0, 2, 1, 3).reshape(B, Sq, Hq * Dh) @ wo

    def _B_ref(x, w1, w2):
        h = torch.einsum("bsd,df->bsf", x, w1)
        h = _silu(h)
        out = torch.einsum("bsf,fd->bsd", h, w2)
        return out

    x = A_x
    xn = _layernorm(x, axis=-1)
    return x + _A_ref(xn, A_k_cache, A_v_cache, A_wq, A_wo) + _B_ref(xn, B_w1, B_w2)
```

```cpp
#include <hip/hip_runtime.h>
#include <hip/hip_bf16.h>
#define MIMIC 0
typedef unsigned short u16;
__device__ __forceinline__ float bf2f(u16 v){ return __uint_as_float(((unsigned)v)<<16); }
__device__ __forceinline__ u16 f2bf(float f){ unsigned u=__float_as_uint(f); u += 0x7FFFu + ((u>>16)&1u); return (u16)(u>>16); }
__device__ __forceinline__ float rb(float f){ return MIMIC ? bf2f(f2bf(f)) : f; }

constexpr int B=8, D=512, HQ=32, HKV=8, SK=4096, DH=128, W=1024, F=1792;

__global__ void k1(const u16* x, const u16* wq, const u16* w1, float* ws){
  __shared__ float xn[B][D];
  int tid=threadIdx.x;
  int wave=tid>>6, lane=tid&63;
  for(int r=wave*2;r<wave*2+2;r++){
    float v[8]; float s=0;
    for(int j=0;j<8;j++){ v[j]=bf2f(x[r*D+lane*8+j]); s+=v[j]; }
    for(int o=32;o>0;o>>=1) s+=__shfl_xor(s,o);
    float mean=s/D;
    float ss=0; for(int j=0;j<8;j++){ float d=v[j]-mean; ss+=d*d; }
    for(int o=32;o>0;o>>=1) ss+=__shfl_xor(ss,o);
    float var=rb(ss/D); float mb=rb(mean);
    float den=rb(sqrtf(rb(var+1e-5f)));
    for(int j=0;j<8;j++){ xn[r][lane*8+j]=bf2f(f2bf(rb(v[j]-mb)/den)); }
  }
  __syncthreads();
  int col=blockIdx.x*256+tid;
  if(col<HQ*DH){
    float acc[B]={0,0,0,0,0,0,0,0};
    for(int k=0;k<D;k++){ float w=bf2f(wq[(size_t)k*(HQ*DH)+col]); for(int b=0;b<B;b++) acc[b]+=xn[b][k]*w; }
    for(int b=0;b<B;b++) ws[B*D + b*HQ*DH+col]=rb(acc[b]);
  } else if(col<HQ*DH+F){
    int c=col-HQ*DH;
    float acc[B]={0,0,0,0,0,0,0,0};
    for(int k=0;k<D;k++){ float w=bf2f(w1[(size_t)k*F+c]); for(int b=0;b<B;b++) acc[b]+=xn[b][k]*w; }
    for(int b=0;b<B;b++){ float h=rb(acc[b]); float s=h/(1.f+__expf(-h)); ws[B*D+B*HQ*DH+b*F+c]=rb(s); }
  }
}
__global__ void k2(const u16* kc, const u16* vc, float* ws){
  int b=blockIdx.x/HQ, h=blockIdx.x%HQ, hk=h/4; int tid=threadIdx.x;
  __shared__ float q[DH]; __shared__ float p[W]; __shared__ float red[256];
  const float* qg=ws+B*D+b*HQ*DH+h*DH;
  if(tid<DH) q[tid]=qg[tid];
  __syncthreads();
  const u16* kb=kc+((size_t)(b*HKV+hk)*SK+(SK-W))*DH;
  const u16* vb=vc+((size_t)(b*HKV+hk)*SK+(SK-W))*DH;
  float mx=-1e30f;
  for(int j=tid;j<W;j+=256){ float s=0; for(int d=0;d<DH;d++) s+=q[d]*bf2f(kb[(size_t)j*DH+d]); s=rb(rb(s)*0.08838834764831845f); p[j]=s; mx=fmaxf(mx,s);}
  red[tid]=mx; __syncthreads();
  for(int o=128;o>0;o>>=1){ if(tid<o) red[tid]=fmaxf(red[tid],red[tid+o]); __syncthreads(); }
  mx=red[0]; __syncthreads();
  float sum=0; for(int j=tid;j<W;j+=256){ float e=__expf(p[j]-mx); p[j]=e; sum+=e; }
  red[tid]=sum; __syncthreads();
  for(int o=128;o>0;o>>=1){ if(tid<o) red[tid]+=red[tid+o]; __syncthreads(); }
  sum=red[0]; __syncthreads();
  for(int j=tid;j<W;j+=256) p[j]=rb(p[j]/sum);
  __syncthreads();
  if(tid<DH){ float o=0; for(int j=0;j<W;j++) o+=p[j]*bf2f(vb[(size_t)j*DH+tid]); ws[B*D+B*HQ*DH+B*F+b*HQ*DH+h*DH+tid]=rb(o); }
}
__global__ void k3(const u16* x, const u16* wo, const u16* w2, const float* ws, u16* out){
  int b=blockIdx.x; int c=blockIdx.y*64+(threadIdx.x&63); int part=threadIdx.x>>6;
  __shared__ float ra[4][64], rbb[4][64];
  const float* o=ws+B*D+B*HQ*DH+B*F+b*HQ*DH; const float* h=ws+B*D+B*HQ*DH+b*F;
  float a=0; for(int k=part*1024;k<part*1024+1024;k++) a+=o[k]*bf2f(wo[(size_t)k*D+c]);
  float f=0; for(int k=part*448;k<part*448+448;k++) f+=h[k]*bf2f(w2[(size_t)k*D+c]);
  ra[part][threadIdx.x&63]=a; rbb[part][threadIdx.x&63]=f; __syncthreads();
  if(part==0){ int l=threadIdx.x; a=ra[0][l]+ra[1][l]+ra[2][l]+ra[3][l]; f=rbb[0][l]+rbb[1][l]+rbb[2][l]+rbb[3][l];
    float r=rb(bf2f(x[b*D+c])+rb(a)); r=r+rb(f); out[b*D+c]=f2bf(r); }
}
extern "C" void kernel_launch(void* const* d_in, const int* in_sizes, int n_in, void* d_out, int out_size, void* d_ws, size_t ws_size, hipStream_t stream){
  const u16* x=(const u16*)d_in[0]; const u16* kc=(const u16*)d_in[1]; const u16* vc=(const u16*)d_in[2];
  const u16* wq=(const u16*)d_in[3]; const u16* wo=(const u16*)d_in[4]; const u16* w1=(const u16*)d_in[6]; const u16* w2=(const u16*)d_in[7];
  float* ws=(float*)d_ws;
  k1<<<(HQ*DH+F)/256,256,0,stream>>>(x,wq,w1,ws);
  k2<<<B*HQ,256,0,stream>>>(kc,vc,ws);
  k3<<<dim3(B,D/64),256,0,stream>>>(x,wo,w2,ws,(u16*)d_out);
}
```

```cpp
#include <hip/hip_runtime.h>
#include <hip/hip_bf16.h>

namespace fused {
typedef unsigned u32; typedef unsigned long long u64; typedef unsigned short u16;
typedef u32 v4u __attribute__((ext_vector_type(4)));
typedef u32 v2u __attribute__((ext_vector_type(2)));
typedef float v4f __attribute__((ext_vector_type(4)));
typedef float v2f __attribute__((ext_vector_type(2)));
typedef __bf16 bf8 __attribute__((ext_vector_type(8)));
typedef short s4 __attribute__((ext_vector_type(4)));
#define LAS __attribute__((address_space(3)))
#define GAS __attribute__((address_space(1)))

constexpr int DM = 512, SK = 4096, DH = 128, WINK = 1024, FF = 1792, NQ = 4096;
constexpr int NWAVE = 9, NTHR = NWAVE * 64;
constexpr int AUX_NT = 2, AUX_SC1 = 16;
constexpr u32 RSRC3 = 0x00020000u;
constexpr u32 SPIN_CAP = 1u << 16;

constexpr u32 WS_EPOCH = 0;
constexpr u32 WS_QG = 0x1000;
constexpr u32 WS_HG = 0x41000;
constexpr u32 WS_ST = 0x50000;
constexpr u32 WS_PO = 0x60000;
constexpr u32 WS_P2 = 0xE0000;
constexpr u32 WS_END = 0x1E0000;
constexpr u32 WS_STAMP = 0x200000;

constexpr int L_XN = 0, XN_STR = 1040;
constexpr int L_WT = 8448;
constexpr int L_RED1 = L_WT + 32768;
constexpr int L_QL = L_RED1 + 16384, QL_STR = 320;
constexpr int L_MX = L_QL + 1280;
constexpr int L_TAG = L_MX + 256;
constexpr int L_OA = L_TAG + 16, OA_STR = 272;
constexpr int L_K = 61440;
constexpr int L_BYTES = L_K + 65536;
static_assert(L_OA + 8 * OA_STR + 96 <= L_K, "LDS map");

typedef __bf16 bf2 __attribute__((ext_vector_type(2)));
__device__ __forceinline__ u32 cvtpk(float lo, float hi) { return __builtin_bit_cast(u32, __builtin_convertvector((v2f{lo, hi}), bf2)); }
__device__ __forceinline__ float bflo(u32 p) { return __builtin_bit_cast(float, p << 16); }
__device__ __forceinline__ float bfhi(u32 p) { return __builtin_bit_cast(float, p & 0xffff0000u); }
__device__ __forceinline__ float rbf(float x) { return bflo(cvtpk(x, x)); }
template <int CTRL> __device__ __forceinline__ float dpp(float x) {
  return __builtin_bit_cast(float, __builtin_amdgcn_mov_dpp(__builtin_bit_cast(int, x), CTRL, 0xf, 0xf, true));
}
constexpr int XOR1 = 0xB1, XOR2 = 0x4E, HMIR = 0x141, RMIR = 0x140, ROR8 = 0x128;
__device__ __forceinline__ float x16_sum(float x) {
  auto s = __builtin_amdgcn_permlane16_swap(__float_as_uint(x), __float_as_uint(x), false, false);
  x = __uint_as_float(s[0]) + __uint_as_float(s[1]);
  auto t = __builtin_amdgcn_permlane32_swap(__float_as_uint(x), __float_as_uint(x), false, false);
  return __uint_as_float(t[0]) + __uint_as_float(t[1]);
}
__device__ __forceinline__ float x16_max(float x) {
  auto s = __builtin_amdgcn_permlane16_swap(__float_as_uint(x), __float_as_uint(x), false, false);
  x = fmaxf(__uint_as_float(s[0]), __uint_as_float(s[1]));
  auto t = __builtin_amdgcn_permlane32_swap(__float_as_uint(x), __float_as_uint(x), false, false);
  return fmaxf(__uint_as_float(t[0]), __uint_as_float(t[1]));
}
__device__ __forceinline__ float wave_sum(float x) {
  x += dpp<XOR1>(x); x += dpp<XOR2>(x); x += dpp<HMIR>(x); x += dpp<RMIR>(x); return x16_sum(x);
}
__device__ __forceinline__ v4f mfma32(v4u a, v4u b, v4f c) {
  return __builtin_amdgcn_mfma_f32_16x16x32_bf16(__builtin_bit_cast(bf8, a), __builtin_bit_cast(bf8, b), c, 0, 0, 0);
}
__device__ __forceinline__ v2u trread(LAS unsigned char* p) {
  typedef short v4s __attribute__((ext_vector_type(4)));
  return __builtin_bit_cast(v2u, __builtin_amdgcn_ds_read_tr16_b64_v4i16((LAS v4s*)p));
}
#define BAR() do { asm volatile("s_waitcnt lgkmcnt(0)" ::: "memory"); __builtin_amdgcn_s_barrier(); asm volatile("" ::: "memory"); } while (0)
#define SB() __builtin_amdgcn_sched_barrier(0)
__device__ __forceinline__ __amdgpu_buffer_rsrc_t mkrs(const void* p, u32 bytes) { return __builtin_amdgcn_make_buffer_rsrc((void*)p, 0, (int)bytes, RSRC3); }
__device__ __forceinline__ v4u ld128(__amdgpu_buffer_rsrc_t r, u32 vo, u32 so, int aux_is_nt) { return __builtin_amdgcn_raw_buffer_load_b128(r, vo, so, AUX_NT); }
#define LDNT128(r, vo, so) __builtin_amdgcn_raw_buffer_load_b128((r), (vo), (so), AUX_NT)
#define LDNT64(r, vo, so) __builtin_amdgcn_raw_buffer_load_b64((r), (vo), (so), AUX_NT)
#define LDSC128(r, vo, so) __builtin_amdgcn_raw_buffer_load_b128((r), (vo), (so), AUX_SC1)
#define STSC128(v, r, vo, so) __builtin_amdgcn_raw_buffer_store_b128((v), (r), (vo), (so), AUX_SC1)

#define STAMP(i) do { } while (0)

struct Args { const u16* x; const u16* kc; const u16* vc; const u16* wq; const u16* wo; const u16* w1; const u16* w2; u16* out; unsigned char* ws; };

__global__ void __launch_bounds__(NTHR, 3) fused_decode(Args a) {
  __shared__ __attribute__((aligned(16))) unsigned char smem[L_BYTES];
  LAS unsigned char* L = (LAS unsigned char*)smem;
  STAMP(0);
  const int tid = threadIdx.x, lane = tid & 63, w = __builtin_amdgcn_readfirstlane(tid >> 6);
  const int bid = blockIdx.x, hkv = bid & 7, j = bid >> 3;
  const int g = lane >> 4, c = lane & 15;
  const int hh = 4 * hkv + (j & 3), cs = j >> 2;
  const bool ffn2 = hh < 14, ffn1 = (hh >= 14) && (hh < 28);
  asm volatile("" :: "s"(a.x), "s"(a.kc), "s"(a.vc), "s"(a.wq), "s"(a.wo), "s"(a.w1), "s"(a.w2), "s"(a.out), "s"(a.ws));
  const __amdgpu_buffer_rsrc_t rws = mkrs(a.ws, WS_END);
  const __amdgpu_buffer_rsrc_t rx = mkrs(a.x, 8192);
  const v4u xr = __builtin_amdgcn_raw_buffer_load_b128(mkrs(w == 8 ? (const void*)a.ws : (const void*)a.x, 8192), w == 8 ? WS_EPOCH : w * 1024 + lane * 16, 0, AUX_SC1);
  STAMP(1);
  SB(); __builtin_amdgcn_s_barrier(); SB();
  v4u wqr[2], w1r[2];
  {
    const __amdgpu_buffer_rsrc_t rwq = mkrs(a.wq, w < 8 ? DM * NQ * 2 : 0);
    const __amdgpu_buffer_rsrc_t rw1 = mkrs(a.w1, (ffn1 && w < 8) ? DM * FF * 2 : 0);
    const u32 vo = (64 * w + (lane >> 1)) * (NQ * 2) + (hkv * 512 + j * 16) * 2 + (lane & 1) * 16;
    wqr[0] = LDNT128(rwq, vo, 0); wqr[1] = LDNT128(rwq, vo, 32 * NQ * 2);
    const int t1 = ffn1 ? (hh - 14) * 8 + cs : 0;
    const u32 vo1 = (64 * w + (lane >> 1)) * (FF * 2) + t1 * 32 + (lane & 1) * 16;
    w1r[0] = LDNT128(rw1, vo1, 0); w1r[1] = LDNT128(rw1, vo1, 32 * FF * 2);
  }
  SB(); __builtin_amdgcn_s_barrier(); SB();

  if (w == 8) {
    const u32 tag = __builtin_amdgcn_readfirstlane(xr.x) + 1u;
    if (lane == 0) *(LAS u32*)(L + L_TAG) = tag;
    BAR();
    BAR();
    STAMP(2);
    {
      const int isH = lane >> 5;
      const int pb = isH ? (lane & 7) : (lane >> 2), pg = isH ? ((lane >> 3) & 3) : (lane & 3);
      LAS v4f* red = (LAS v4f*)(L + L_RED1 + isH * 8192) + (pg * 16 + pb);
      v4f s = red[0];
#pragma unroll
      for (int ww = 1; ww < 8; ++ww) s += red[ww * 64];
      v4u o;
      if (!isH) {
        o.x = cvtpk(s.x, s.y); o.z = cvtpk(s.z, s.w); o.y = tag; o.w = tag;
        STSC128(o, rws, WS_QG + ((pb * 8 + hkv) * 32 + j) * 128 + pg * 16, 0);
      } else if (ffn1) {
        float hv[4];
#pragma unroll
        for (int r = 0; r < 4; ++r) { const float hb = rbf(s[r]); hv[r] = hb * __builtin_amdgcn_rcpf(1.0f + __builtin_amdgcn_exp2f(-1.4426950408889634f * hb)); }
        o.x = cvtpk(hv[0], hv[1]); o.z = cvtpk(hv[2], hv[3]); o.y = tag; o.w = tag;
        const int t1 = (hh - 14) * 8 + cs;
        STSC128(o, rws, WS_HG + t1 * 512 + (lane - 32) * 16, 0);
      }
    }
    STAMP(3);
    {
      const int b = j >> 2;
      const u32 off = WS_QG + ((b * 8 + hkv) * 32 + (lane >> 2)) * 128 + (lane & 3) * 16;
      v4u q0, q1;
      for (u32 spin = 0; spin < SPIN_CAP; ++spin) {
        asm volatile("" ::: "memory");
        q0 = LDSC128(rws, off, 0); q1 = LDSC128(rws, off + 16 * 128, 0);
        const bool ok = (q0.y == tag) & (q0.w == tag) & (q1.y == tag) & (q1.w == tag);
        if (__all(ok)) break;
      }
      LAS unsigned char* ql = L + L_QL + (lane >> 5) * QL_STR + ((lane >> 2) & 7) * 32 + (lane & 3) * 8;
      *(LAS v2u*)ql = v2u{q0.x, q0.z};
      *(LAS v2u*)(ql + 2 * QL_STR) = v2u{q1.x, q1.z};
    }
    STAMP(4);
    BAR();
    return;
  }

  const int b = j >> 2, s = j & 3;
  const size_t kvbase = ((size_t)(b * 8 + hkv) * SK + (SK - WINK) + s * 256 + w * 32) * DH * 2;
  const __amdgpu_buffer_rsrc_t rk = mkrs((const unsigned char*)a.kc + kvbase, 8192);
  const __amdgpu_buffer_rsrc_t rv = mkrs((const unsigned char*)a.vc + kvbase, 8192);
  const __amdgpu_buffer_rsrc_t rwo = mkrs(a.wo, NQ * DM * 2);
  const __amdgpu_buffer_rsrc_t rw2 = mkrs(a.w2, ffn2 ? FF * DM * 2 : 0);
  v4u K[4], V[8];
  const u32 vvo = g * 1024 + c * 16;
  const u32 kvo = g * 256 + c * 16;
  LAS unsigned char* kl = L + L_K + w * 8192;
#define KLD(u) K[(u) & 3] = LDNT128(rk, kvo + ((u) & 3) * 1024, ((u) >> 2) * 4096)
#define KST(u) *(LAS v4u*)(kl + (4 * (u) + g) * 256 + ((c ^ ((4 * (u) + g) & 15)) * 16)) = K[(u) & 3]
#define VLD(i) V[i] = LDNT128(rv, vvo + ((i) & 3) * 256, ((i) >> 2) * 4096)
  KLD(0); KLD(1); KLD(2); KLD(3);
  SB();
  {
    float xv[8];
#pragma unroll
    for (int i = 0; i < 4; ++i) { xv[2 * i] = bflo(xr[i]); xv[2 * i + 1] = bfhi(xr[i]); }
    float sm = ((xv[0] + xv[1]) + (xv[2] + xv[3])) + ((xv[4] + xv[5]) + (xv[6] + xv[7]));
    sm = wave_sum(sm);
    const float mean = sm * (1.0f / DM);
    float ss = 0.f;
#pragma unroll
    for (int i = 0; i < 8; ++i) { const float d = xv[i] - mean; ss = fmaf(d, d, ss); }
    ss = wave_sum(ss);
    const float mb = rbf(mean), var = rbf(ss * (1.0f / DM));
    const float den = rbf(__builtin_amdgcn_sqrtf(rbf(var + 1e-5f)));
    const float inv = __builtin_amdgcn_rcpf(den);
    v4u o;
#pragma unroll
    for (int i = 0; i < 4; ++i) {
      const u32 dd = cvtpk(xv[2 * i] - mb, xv[2 * i + 1] - mb);
      o[i] = cvtpk(bflo(dd) * inv, bfhi(dd) * inv);
    }
    *(LAS v4u*)(L + L_XN + w * XN_STR + lane * 16) = o;
  }
  STAMP(2);
  BAR();
  {
    LAS unsigned char* wt = L + L_WT + w * 2048;
    *(LAS v4u*)(wt + lane * 16) = wqr[0]; *(LAS v4u*)(wt + 1024 + lane * 16) = wqr[1];
    *(LAS v4u*)(wt + 16384 + lane * 16) = w1r[0]; *(LAS v4u*)(wt + 16384 + 1024 + lane * 16) = w1r[1];
    asm volatile("s_waitcnt lgkmcnt(0)" ::: "memory");
    v4u xb[2];
#pragma unroll
    for (int t = 0; t < 2; ++t) {
      LAS unsigned char* xp = L + L_XN + (c & 7) * XN_STR + (64 * w + 32 * t + 4 * g) * 2;
      const v2u lo = *(LAS v2u*)xp, hi = *(LAS v2u*)(xp + 32);
      xb[t] = v4u{lo.x, lo.y, hi.x, hi.y};
    }
    v4f aq = {0.f, 0.f, 0.f, 0.f}, ah = {0.f, 0.f, 0.f, 0.f};
#pragma unroll
    for (int t = 0; t < 2; ++t) {
      const v2u q0 = trread(wt + t * 1024 + lane * 8), q1 = trread(wt + t * 1024 + 512 + lane * 8);
      aq = mfma32(v4u{q0.x, q0.y, q1.x, q1.y}, xb[t], aq);
      const v2u h0 = trread(wt + 16384 + t * 1024 + lane * 8), h1 = trread(wt + 16384 + t * 1024 + 512 + lane * 8);
      ah = mfma32(v4u{h0.x, h0.y, h1.x, h1.y}, xb[t], ah);
    }
    *(LAS v4f*)(L + L_RED1 + w * 1024 + lane * 16) = aq;
    *(LAS v4f*)(L + L_RED1 + 8192 + w * 1024 + lane * 16) = ah;
  }
  STAMP(3);
  BAR();
#define PIN(x) asm volatile("" : "+v"(x))
  KST(0); SB(); KLD(4); SB(); KST(1); SB(); KLD(5); SB(); KST(2); SB(); KLD(6); SB(); KST(3); SB(); KLD(7); SB();
  KST(4); SB(); VLD(0); SB(); KST(5); SB(); VLD(1); SB(); KST(6); SB(); VLD(2); SB(); KST(7); SB(); VLD(3); SB();
  PIN(V[0]); VLD(4); SB(); PIN(V[1]); VLD(5); SB(); PIN(V[2]); VLD(6); SB(); PIN(V[3]); VLD(7); SB();
  v4u wor[2], w2r[2];
  {
    const u32 vo = (hh * 128 + 16 * w + (lane >> 3)) * (DM * 2) + cs * 128 + (lane & 7) * 16;
    const u32 vo2 = ((ffn2 ? hh : 0) * 128 + 16 * w + (lane >> 3)) * (DM * 2) + cs * 128 + (lane & 7) * 16;
    wor[0] = LDNT128(rwo, vo, 0); wor[1] = LDNT128(rwo, vo, 8 * DM * 2);
    w2r[0] = LDNT128(rw2, vo2, 0); w2r[1] = LDNT128(rw2, vo2, 8 * DM * 2);
  }
  SB();
  const u32 tag = *(LAS u32*)(L + L_TAG);
  STAMP(4);
  BAR();
  STAMP(5);
  float sc[8];
  {
    v4u qf[4];
#pragma unroll
    for (int i = 0; i < 4; ++i) qf[i] = *(LAS v4u*)(L + L_QL + (c >> 2) * QL_STR + i * 64 + g * 16);
    v4f s0 = {0.f, 0.f, 0.f, 0.f}, s1 = s0;
#pragma unroll
    for (int i = 0; i < 4; ++i) {
      const v4u k0 = *(LAS v4u*)(kl + c * 256 + (((4 * i + g) ^ c) * 16)), k1 = *(LAS v4u*)(kl + 4096 + c * 256 + (((4 * i + g) ^ c) * 16));
      s0 = mfma32(k0, qf[i], s0); s1 = mfma32(k1, qf[i], s1);
    }
    constexpr float SCALE = 0.08838834764831845f;
#pragma unroll
    for (int r = 0; r < 4; r += 2) {
      const u32 p0 = cvtpk(s0[r], s0[r + 1]), p1 = cvtpk(s1[r], s1[r + 1]);
      const u32 t0 = cvtpk(bflo(p0) * SCALE, bfhi(p0) * SCALE), t1 = cvtpk(bflo(p1) * SCALE, bfhi(p1) * SCALE);
      sc[r] = bflo(t0); sc[r + 1] = bfhi(t0); sc[4 + r] = bflo(t1); sc[4 + r + 1] = bfhi(t1);
    }
  }
  float mx = fmaxf(fmaxf(fmaxf(sc[0], sc[1]), fmaxf(sc[2], sc[3])), fmaxf(fmaxf(sc[4], sc[5]), fmaxf(sc[6], sc[7])));
  mx = x16_max(mx);
  LAS float* mxl = (LAS float*)(L + L_MX);
  if (g == 0 && (c & 3) == 0) mxl[(c >> 2) * 8 + w] = mx;
  BAR();
  float M;
  {
    const v4f m0 = *(LAS v4f*)(mxl + (c >> 2) * 8), m1 = *(LAS v4f*)(mxl + (c >> 2) * 8 + 4);
    M = fmaxf(fmaxf(fmaxf(m0.x, m0.y), fmaxf(m0.z, m0.w)), fmaxf(fmaxf(m1.x, m1.y), fmaxf(m1.z, m1.w)));
  }
  v4u P;
  {
    constexpr float L2E = 1.4426950408889634f;
    float e[8], ls = 0.f;
#pragma unroll
    for (int i = 0; i < 8; ++i) { e[i] = __builtin_amdgcn_exp2f((sc[i] - M) * L2E); ls += e[i]; }
    P = v4u{cvtpk(e[0], e[1]), cvtpk(e[2], e[3]), cvtpk(e[4], e[5]), cvtpk(e[6], e[7])};
    ls = x16_sum(ls);
    if (g == 0 && (c & 3) == 0) mxl[32 + (c >> 2) * 8 + w] = ls;
  }
  STAMP(6);
  {
    v4f o[8];
#pragma unroll
    for (int jj = 0; jj < 8; ++jj) {
      const u32 sel = (jj & 1) ? 0x07060302u : 0x05040100u;
      v4u bf;
#pragma unroll
      for (int pr = 0; pr < 4; ++pr) bf[pr] = __builtin_amdgcn_perm(V[2 * pr + 1][jj >> 1], V[2 * pr][jj >> 1], sel);
      o[jj] = mfma32(P, bf, v4f{0.f, 0.f, 0.f, 0.f});
    }
    LAS unsigned char* r2 = L + L_WT + w * 2048 + g * 512 + c * 32;
    *(LAS v4f*)r2 = v4f{o[0][0], o[1][0], o[2][0], o[3][0]};
    *(LAS v4f*)(r2 + 16) = v4f{o[4][0], o[5][0], o[6][0], o[7][0]};
  }
  STAMP(7);
  BAR();
  if (tid < 128) {
    const int hd = tid >> 5, dq = tid & 31;
    LAS v4f* r2 = (LAS v4f*)(L + L_WT + tid * 16);
    v4f o = r2[0];
#pragma unroll
    for (int ww = 1; ww < 8; ++ww) o += r2[ww * 128];
    const v4f l0 = *(LAS v4f*)(mxl + 32 + hd * 8), l1 = *(LAS v4f*)(mxl + 32 + hd * 8 + 4);
    const float Ls = ((l0.x + l0.y) + (l0.z + l0.w)) + ((l1.x + l1.y) + (l1.z + l1.w));
    const float inv = __builtin_amdgcn_rcpf(Ls);
    const u32 h32 = 4 * hkv + hd;
    STSC128((v4u{cvtpk(o.x * inv, o.y * inv), tag, cvtpk(o.z * inv, o.w * inv), tag}), rws, WS_PO + ((h32 * 8 + b) * 4 + s) * 512 + dq * 16, 0);
  }
  if (w == 2 && lane < 4) {
    const v4f l0 = *(LAS v4f*)(mxl + 32 + lane * 8), l1 = *(LAS v4f*)(mxl + 32 + lane * 8 + 4);
    const float Ls = ((l0.x + l0.y) + (l0.z + l0.w)) + ((l1.x + l1.y) + (l1.z + l1.w));
    const v4f m0 = *(LAS v4f*)(mxl + lane * 8), m1 = *(LAS v4f*)(mxl + lane * 8 + 4);
    const float Mh = fmaxf(fmaxf(fmaxf(m0.x, m0.y), fmaxf(m0.z, m0.w)), fmaxf(fmaxf(m1.x, m1.y), fmaxf(m1.z, m1.w)));
    STSC128((v4u{__float_as_uint(Mh), tag, __float_as_uint(Ls), tag}), rws, WS_ST + ((hkv * 8 + b) * 4 + s) * 128 + lane * 16, 0);
  }
  STAMP(8);
  {
    LAS unsigned char* wl = L + L_K + w * 8192;
    *(LAS v4u*)(wl + (lane >> 3) * 160 + (lane & 7) * 16) = wor[0]; *(LAS v4u*)(wl + (8 + (lane >> 3)) * 160 + (lane & 7) * 16) = wor[1];
    *(LAS v4u*)(wl + 2560 + (lane >> 3) * 160 + (lane & 7) * 16) = w2r[0]; *(LAS v4u*)(wl + 2560 + (8 + (lane >> 3)) * 160 + (lane & 7) * 16) = w2r[1];
    asm volatile("s_waitcnt lgkmcnt(0)" ::: "memory");
    v4u wa[4];
#pragma unroll
    for (int ct = 0; ct < 4; ++ct) {
      LAS unsigned char* tp = wl + (4 * g + (c >> 2)) * 160 + ct * 32 + (c & 3) * 8;
      const v2u t0 = trread(tp), t1 = trread(tp + 2560);
      wa[ct] = v4u{t0.x, t0.y, t1.x, t1.y};
    }
    const u32 poff = WS_PO + ((hh * 8 + w) * 4) * 512 + lane * 16;
    const u32 soff = WS_ST + ((hkv * 8 + w) * 4 + (lane >> 5)) * 128 + (hh & 3) * 16;
    const u32 hoff = WS_HG + ((ffn2 ? hh : 0) * 8 + w) * 512 + g * 128 + (c & 7) * 16;
    v4u po[2], st[2], hg = {0u, tag, 0u, tag};
    for (u32 spin = 0; spin < SPIN_CAP; ++spin) {
      asm volatile("" ::: "memory");
#pragma unroll
      for (int i = 0; i < 2; ++i) { po[i] = LDSC128(rws, poff + i * 1024, 0); st[i] = LDSC128(rws, soff + i * 256, 0); }
      if (ffn2) hg = LDSC128(rws, hoff, 0);
      bool ok = (hg.y == tag) & (hg.w == tag);
#pragma unroll
      for (int i = 0; i < 2; ++i) ok &= (po[i].y == tag) & (po[i].w == tag) & (st[i].y == tag) & (st[i].w == tag);
      if (__all(ok)) break;
    }
    STAMP(9);
    float Mg = fmaxf(__uint_as_float(st[0].x), __uint_as_float(st[1].x));
    { auto t = __builtin_amdgcn_permlane32_swap(__float_as_uint(Mg), __float_as_uint(Mg), false, false); Mg = fmaxf(__uint_as_float(t[0]), __uint_as_float(t[1])); }
    float ws_[2];
#pragma unroll
    for (int i = 0; i < 2; ++i) ws_[i] = __builtin_amdgcn_exp2f((__uint_as_float(st[i].x) - Mg) * 1.4426950408889634f) * __uint_as_float(st[i].z);
    float W = ws_[0] + ws_[1];
    { auto t = __builtin_amdgcn_permlane32_swap(__float_as_uint(W), __float_as_uint(W), false, false); W = __uint_as_float(t[0]) + __uint_as_float(t[1]); }
    const float iw = __builtin_amdgcn_rcpf(W);
    const float f0 = ws_[0] * iw, f1 = ws_[1] * iw;
    float ov[4] = {fmaf(f0, bflo(po[0].x), f1 * bflo(po[1].x)), fmaf(f0, bfhi(po[0].x), f1 * bfhi(po[1].x)),
                   fmaf(f0, bflo(po[0].z), f1 * bflo(po[1].z)), fmaf(f0, bfhi(po[0].z), f1 * bfhi(po[1].z))};
#pragma unroll
    for (int i = 0; i < 4; ++i) { auto t = __builtin_amdgcn_permlane32_swap(__float_as_uint(ov[i]), __float_as_uint(ov[i]), false, false); ov[i] = __uint_as_float(t[0]) + __uint_as_float(t[1]); }
    if (lane < 32) *(LAS v2u*)(L + L_OA + w * OA_STR + lane * 8) = v2u{cvtpk(ov[0], ov[1]), cvtpk(ov[2], ov[3])};
    BAR();
    const v2u oa = *(LAS v2u*)(L + L_OA + (c & 7) * OA_STR + (16 * w + 4 * g) * 2);
    const v4u Bf = {oa.x, oa.y, hg.x, hg.z};
    LAS unsigned char* r3 = L + L_WT + 16384 + w * 2176;
#pragma unroll
    for (int ct = 0; ct < 4; ++ct) {
      const v4f d = mfma32(wa[ct], Bf, v4f{0.f, 0.f, 0.f, 0.f});
      if (c < 8) *(LAS v4f*)(r3 + c * 272 + ct * 64 + g * 16) = d;
    }
  }
  BAR();
  if (tid < 256) {
    LAS v2f* r3 = (LAS v2f*)(L + L_WT + 16384 + (tid >> 5) * 272 + (tid & 31) * 8);
    v2f o = r3[0];
#pragma unroll
    for (int ww = 1; ww < 8; ++ww) o += r3[ww * 272];
    const int pb = tid >> 5, col = cs * 64 + (tid & 31) * 2;
    const u32 off = WS_P2 + (((pb * 32 + (col >> 4)) * 32 + hh) * 16 + (col & 15)) * 8;
    STSC128((v4u{__float_as_uint(o.x), tag, __float_as_uint(o.y), tag}), rws, off, 0);
  }
  STAMP(10);
  if (w == 0) {
    const int fb = bid & 7, c16 = bid >> 3;
    const u32 xo = fb * 1024 + c16 * 32 + (lane & 7) * 4;
    const u32 xp = __builtin_amdgcn_raw_buffer_load_b32(rx, xo, 0, 0);
    const u32 off = WS_P2 + ((fb * 32 + c16) * 32 + (lane >> 3)) * 128 + (lane & 7) * 16;
    v4u p[4];
    for (u32 spin = 0; spin < SPIN_CAP; ++spin) {
      asm volatile("" ::: "memory");
#pragma unroll
      for (int i = 0; i < 4; ++i) p[i] = LDSC128(rws, off + i * 1024, 0);
      bool ok = true;
#pragma unroll
      for (int i = 0; i < 4; ++i) ok &= (p[i].y == tag) & (p[i].w == tag);
      if (__all(ok)) break;
    }
    float s0 = (__uint_as_float(p[0].x) + __uint_as_float(p[1].x)) + (__uint_as_float(p[2].x) + __uint_as_float(p[3].x));
    float s1 = (__uint_as_float(p[0].z) + __uint_as_float(p[1].z)) + (__uint_as_float(p[2].z) + __uint_as_float(p[3].z));
    s0 += dpp<ROR8>(s0); s1 += dpp<ROR8>(s1);
    s0 = x16_sum(s0); s1 = x16_sum(s1);
    if (lane < 8) ((GAS u32*)a.out)[(fb * 512 + c16 * 16) / 2 + lane] = cvtpk(bflo(xp) + s0, bfhi(xp) + s1);
    if (bid == 0 && lane == 0) __hip_atomic_store((GAS u32*)(a.ws + WS_EPOCH), tag, __ATOMIC_RELAXED, __HIP_MEMORY_SCOPE_AGENT);
  }
  STAMP(11);
}
}

extern "C" void kernel_launch(void* const* d_in, const int* in_sizes, int n_in, void* d_out, int out_size, void* d_ws, size_t ws_size, hipStream_t stream) {
  using namespace fused;
  Args a;
  a.x = (const u16*)d_in[0]; a.kc = (const u16*)d_in[1]; a.vc = (const u16*)d_in[2]; a.wq = (const u16*)d_in[3]; a.wo = (const u16*)d_in[4];
  a.w1 = (const u16*)d_in[6]; a.w2 = (const u16*)d_in[7]; a.out = (u16*)d_out; a.ws = (unsigned char*)d_ws;
  hipLaunchKernelGGL(fused_decode, dim3(256), dim3(NTHR), 0, stream, a);
}
```

```cpp
#define PROBE 1
#define PROBE_BLK 0
#include <hip/hip_runtime.h>
#include <hip/hip_bf16.h>

namespace fused {
typedef unsigned u32; typedef unsigned long long u64; typedef unsigned short u16;
typedef u32 v4u __attribute__((ext_vector_type(4)));
typedef u32 v2u __attribute__((ext_vector_type(2)));
typedef float v4f __attribute__((ext_vector_type(4)));
typedef float v2f __attribute__((ext_vector_type(2)));
typedef __bf16 bf8 __attribute__((ext_vector_type(8)));
typedef short s4 __attribute__((ext_vector_type(4)));
#define LAS __attribute__((address_space(3)))
#define GAS __attribute__((address_space(1)))

constexpr int DM = 512, SK = 4096, DH = 128, WINK = 1024, FF = 1792, NQ = 4096;
constexpr int NWAVE = 9, NTHR = NWAVE * 64;
constexpr int AUX_NT = 2, AUX_SC1 = 16;
constexpr u32 RSRC3 = 0x00020000u;
constexpr u32 SPIN_CAP = 1u << 16;

constexpr u32 WS_EPOCH = 0;
constexpr u32 WS_QG = 0x1000;
constexpr u32 WS_HG = 0x41000;
constexpr u32 WS_ST = 0x50000;
constexpr u32 WS_PO = 0x60000;
constexpr u32 WS_P2 = 0xE0000;
constexpr u32 WS_END = 0x1E0000;
constexpr u32 WS_STAMP = 0x200000;

constexpr int L_XN = 0, XN_STR = 1040;
constexpr int L_WT = 8448;
constexpr int L_RED1 = L_WT + 32768;
constexpr int L_QL = L_RED1 + 16384, QL_STR = 320;
constexpr int L_MX = L_QL + 1280;
constexpr int L_TAG = L_MX + 256;
constexpr int L_OA = L_TAG + 16, OA_STR = 272;
constexpr int L_K = 61440;
constexpr int L_BYTES = L_K + 65536;
static_assert(L_OA + 8 * OA_STR + 96 <= L_K, "LDS map");

typedef __bf16 bf2 __attribute__((ext_vector_type(2)));
__device__ __forceinline__ u32 cvtpk(float lo, float hi) { return __builtin_bit_cast(u32, __builtin_convertvector((v2f{lo, hi}), bf2)); }
__device__ __forceinline__ float bflo(u32 p) { return __builtin_bit_cast(float, p << 16); }
__device__ __forceinline__ float bfhi(u32 p) { return __builtin_bit_cast(float, p & 0xffff0000u); }
__device__ __forceinline__ float rbf(float x) { return bflo(cvtpk(x, x)); }
template <int CTRL> __device__ __forceinline__ float dpp(float x) {
  return __builtin_bit_cast(float, __builtin_amdgcn_mov_dpp(__builtin_bit_cast(int, x), CTRL, 0xf, 0xf, true));
}
constexpr int XOR1 = 0xB1, XOR2 = 0x4E, HMIR = 0x141, RMIR = 0x140, ROR8 = 0x128;
__device__ __forceinline__ float x16_sum(float x) {
  auto s = __builtin_amdgcn_permlane16_swap(__float_as_uint(x), __float_as_uint(x), false, false);
  x = __uint_as_float(s[0]) + __uint_as_float(s[1]);
  auto t = __builtin_amdgcn_permlane32_swap(__float_as_uint(x), __float_as_uint(x), false, false);
  return __uint_as_float(t[0]) + __uint_as_float(t[1]);
}
__device__ __forceinline__ float x16_max(float x) {
  auto s = __builtin_amdgcn_permlane16_swap(__float_as_uint(x), __float_as_uint(x), false, false);
  x = fmaxf(__uint_as_float(s[0]), __uint_as_float(s[1]));
  auto t = __builtin_amdgcn_permlane32_swap(__float_as_uint(x), __float_as_uint(x), false, false);
  return fmaxf(__uint_as_float(t[0]), __uint_as_float(t[1]));
}
__device__ __forceinline__ float wave_sum(float x) {
  x += dpp<XOR1>(x); x += dpp<XOR2>(x); x += dpp<HMIR>(x); x += dpp<RMIR>(x); return x16_sum(x);
}
__device__ __forceinline__ v4f mfma32(v4u a, v4u b, v4f c) {
  return __builtin_amdgcn_mfma_f32_16x16x32_bf16(__builtin_bit_cast(bf8, a), __builtin_bit_cast(bf8, b), c, 0, 0, 0);
}
__device__ __forceinline__ v2u trread(LAS unsigned char* p) {
  typedef short v4s __attribute__((ext_vector_type(4)));
  return __builtin_bit_cast(v2u, __builtin_amdgcn_ds_read_tr16_b64_v4i16((LAS v4s*)p));
}
#define BAR() do { asm volatile("s_waitcnt lgkmcnt(0)" ::: "memory"); __builtin_amdgcn_s_barrier(); asm volatile("" ::: "memory"); } while (0)
#define SB() __builtin_amdgcn_sched_barrier(0)
__device__ __forceinline__ __amdgpu_buffer_rsrc_t mkrs(const void* p, u32 bytes) { return __builtin_amdgcn_make_buffer_rsrc((void*)p, 0, (int)bytes, RSRC3); }
__device__ __forceinline__ v4u ld128(__amdgpu_buffer_rsrc_t r, u32 vo, u32 so, int aux_is_nt) { return __builtin_amdgcn_raw_buffer_load_b128(r, vo, so, AUX_NT); }
#define LDNT128(r, vo, so) __builtin_amdgcn_raw_buffer_load_b128((r), (vo), (so), AUX_NT)
#define LDNT64(r, vo, so) __builtin_amdgcn_raw_buffer_load_b64((r), (vo), (so), AUX_NT)
#define LDSC128(r, vo, so) __builtin_amdgcn_raw_buffer_load_b128((r), (vo), (so), AUX_SC1)
#define STSC128(v, r, vo, so) __builtin_amdgcn_raw_buffer_store_b128((v), (r), (vo), (so), AUX_SC1)

#define STAMP(i) do { if ((i) == 0 || (i) == PROBE) { u64 t_; asm volatile("s_memrealtime %0\n\ts_waitcnt lgkmcnt(0)" : "=s"(t_) :: "memory"); if ((i) == 0) pt0 = t_; else pt1 = t_; } } while (0)

struct Args { const u16* x; const u16* kc; const u16* vc; const u16* wq; const u16* wo; const u16* w1; const u16* w2; u16* out; unsigned char* ws; };

__global__ void __launch_bounds__(NTHR, 3) fused_decode(Args a) {
  __shared__ __attribute__((aligned(16))) unsigned char smem[L_BYTES];
  LAS unsigned char* L = (LAS unsigned char*)smem;
  u64 pt0 = 0, pt1 = 0;
  STAMP(0);
  const int tid = threadIdx.x, lane = tid & 63, w = __builtin_amdgcn_readfirstlane(tid >> 6);
  const int bid = blockIdx.x, hkv = bid & 7, j = bid >> 3;
  const int g = lane >> 4, c = lane & 15;
  const int hh = 4 * hkv + (j & 3), cs = j >> 2;
  const bool ffn2 = hh < 14, ffn1 = (hh >= 14) && (hh < 28);
  asm volatile("" :: "s"(a.x), "s"(a.kc), "s"(a.vc), "s"(a.wq), "s"(a.wo), "s"(a.w1), "s"(a.w2), "s"(a.out), "s"(a.ws));
  const __amdgpu_buffer_rsrc_t rws = mkrs(a.ws, WS_END);
  const __amdgpu_buffer_rsrc_t rx = mkrs(a.x, 8192);
  const v4u xr = __builtin_amdgcn_raw_buffer_load_b128(mkrs(w == 8 ? (const void*)a.ws : (const void*)a.x, 8192), w == 8 ? WS_EPOCH : w * 1024 + lane * 16, 0, AUX_SC1);
  STAMP(1);
  SB(); __builtin_amdgcn_s_barrier(); SB();
  v4u wqr[2], w1r[2];
  {
    const __amdgpu_buffer_rsrc_t rwq = mkrs(a.wq, w < 8 ? DM * NQ * 2 : 0);
    const __amdgpu_buffer_rsrc_t rw1 = mkrs(a.w1, (ffn1 && w < 8) ? DM * FF * 2 : 0);
    const u32 vo = (64 * w + (lane >> 1)) * (NQ * 2) + (hkv * 512 + j * 16) * 2 + (lane & 1) * 16;
    wqr[0] = LDNT128(rwq, vo, 0); wqr[1] = LDNT128(rwq, vo, 32 * NQ * 2);
    const int t1 = ffn1 ? (hh - 14) * 8 + cs : 0;
    const u32 vo1 = (64 * w + (lane >> 1)) * (FF * 2) + t1 * 32 + (lane & 1) * 16;
    w1r[0] = LDNT128(rw1, vo1, 0); w1r[1] = LDNT128(rw1, vo1, 32 * FF * 2);
  }
  SB(); __builtin_amdgcn_s_barrier(); SB();

  if (w == 8) {
    const u32 tag = __builtin_amdgcn_readfirstlane(xr.x) + 1u;
    if (lane == 0) *(LAS u32*)(L + L_TAG) = tag;
    BAR();
    BAR();
    STAMP(2);
    {
      const int isH = lane >> 5;
      const int pb = isH ? (lane & 7) : (lane >> 2), pg = isH ? ((lane >> 3) & 3) : (lane & 3);
      LAS v4f* red = (LAS v4f*)(L + L_RED1 + isH * 8192) + (pg * 16 + pb);
      v4f s = red[0];
#pragma unroll
      for (int ww = 1; ww < 8; ++ww) s += red[ww * 64];
      v4u o;
      if (!isH) {
        o.x = cvtpk(s.x, s.y); o.z = cvtpk(s.z, s.w); o.y = tag; o.w = tag;
        STSC128(o, rws, WS_QG + ((pb * 8 + hkv) * 32 + j) * 128 + pg * 16, 0);
      } else if (ffn1) {
        float hv[4];
#pragma unroll
        for (int r = 0; r < 4; ++r) { const float hb = rbf(s[r]); hv[r] = hb * __builtin_amdgcn_rcpf(1.0f + __builtin_amdgcn_exp2f(-1.4426950408889634f * hb)); }
        o.x = cvtpk(hv[0], hv[1]); o.z = cvtpk(hv[2], hv[3]); o.y = tag; o.w = tag;
        const int t1 = (hh - 14) * 8 + cs;
        STSC128(o, rws, WS_HG + t1 * 512 + (lane - 32) * 16, 0);
      }
    }
    STAMP(3);
    {
      const int b = j >> 2;
      const u32 off = WS_QG + ((b * 8 + hkv) * 32 + (lane >> 2)) * 128 + (lane & 3) * 16;
      v4u q0, q1;
      for (u32 spin = 0; spin < SPIN_CAP; ++spin) {
        asm volatile("" ::: "memory");
        q0 = LDSC128(rws, off, 0); q1 = LDSC128(rws, off + 16 * 128, 0);
        const bool ok = (q0.y == tag) & (q0.w == tag) & (q1.y == tag) & (q1.w == tag);
        if (__all(ok)) break;
      }
      LAS unsigned char* ql = L + L_QL + (lane >> 5) * QL_STR + ((lane >> 2) & 7) * 32 + (lane & 3) * 8;
      *(LAS v2u*)ql = v2u{q0.x, q0.z};
      *(LAS v2u*)(ql + 2 * QL_STR) = v2u{q1.x, q1.z};
    }
    STAMP(4);
    BAR();
    return;
  }

  const int b = j >> 2, s = j & 3;
  const size_t kvbase = ((size_t)(b * 8 + hkv) * SK + (SK - WINK) + s * 256 + w * 32) * DH * 2;
  const __amdgpu_buffer_rsrc_t rk = mkrs((const unsigned char*)a.kc + kvbase, 8192);
  const __amdgpu_buffer_rsrc_t rv = mkrs((const unsigned char*)a.vc + kvbase, 8192);
  const __amdgpu_buffer_rsrc_t rwo = mkrs(a.wo, NQ * DM * 2);
  const __amdgpu_buffer_rsrc_t rw2 = mkrs(a.w2, ffn2 ? FF * DM * 2 : 0);
  v4u K[4], V[8];
  const u32 vvo = g * 1024 + c * 16;
  const u32 kvo = g * 256 + c * 16;
  LAS unsigned char* kl = L + L_K + w * 8192;
#define KLD(u) K[(u) & 3] = LDNT128(rk, kvo + ((u) & 3) * 1024, ((u) >> 2) * 4096)
#define KST(u) *(LAS v4u*)(kl + (4 * (u) + g) * 256 + ((c ^ ((4 * (u) + g) & 15)) * 16)) = K[(u) & 3]
#define VLD(i) V[i] = LDNT128(rv, vvo + ((i) & 3) * 256, ((i) >> 2) * 4096)
  KLD(0); KLD(1); KLD(2); KLD(3);
  SB();
  {
    float xv[8];
#pragma unroll
    for (int i = 0; i < 4; ++i) { xv[2 * i] = bflo(xr[i]); xv[2 * i + 1] = bfhi(xr[i]); }
    float sm = ((xv[0] + xv[1]) + (xv[2] + xv[3])) + ((xv[4] + xv[5]) + (xv[6] + xv[7]));
    sm = wave_sum(sm);
    const float mean = sm * (1.0f / DM);
    float ss = 0.f;
#pragma unroll
    for (int i = 0; i < 8; ++i) { const float d = xv[i] - mean; ss = fmaf(d, d, ss); }
    ss = wave_sum(ss);
    const float mb = rbf(mean), var = rbf(ss * (1.0f / DM));
    const float den = rbf(__builtin_amdgcn_sqrtf(rbf(var + 1e-5f)));
    const float inv = __builtin_amdgcn_rcpf(den);
    v4u o;
#pragma unroll
    for (int i = 0; i < 4; ++i) {
      const u32 dd = cvtpk(xv[2 * i] - mb, xv[2 * i + 1] - mb);
      o[i] = cvtpk(bflo(dd) * inv, bfhi(dd) * inv);
    }
    *(LAS v4u*)(L + L_XN + w * XN_STR + lane * 16) = o;
  }
  STAMP(2);
  BAR();
  {
    LAS unsigned char* wt = L + L_WT + w * 2048;
    *(LAS v4u*)(wt + lane * 16) = wqr[0]; *(LAS v4u*)(wt + 1024 + lane * 16) = wqr[1];
    *(LAS v4u*)(wt + 16384 + lane * 16) = w1r[0]; *(LAS v4u*)(wt + 16384 + 1024 + lane * 16) = w1r[1];
    asm volatile("s_waitcnt lgkmcnt(0)" ::: "memory");
    v4u xb[2];
#pragma unroll
    for (int t = 0; t < 2; ++t) {
      LAS unsigned char* xp = L + L_XN + (c & 7) * XN_STR + (64 * w + 32 * t + 4 * g) * 2;
      const v2u lo = *(LAS v2u*)xp, hi = *(LAS v2u*)(xp + 32);
      xb[t] = v4u{lo.x, lo.y, hi.x, hi.y};
    }
    v4f aq = {0.f, 0.f, 0.f, 0.f}, ah = {0.f, 0.f, 0.f, 0.f};
#pragma unroll
    for (int t = 0; t < 2; ++t) {
      const v2u q0 = trread(wt + t * 1024 + lane * 8), q1 = trread(wt + t * 1024 + 512 + lane * 8);
      aq = mfma32(v4u{q0.x, q0.y, q1.x, q1.y}, xb[t], aq);
      const v2u h0 = trread(wt + 16384 + t * 1024 + lane * 8), h1 = trread(wt + 16384 + t * 1024 + 512 + lane * 8);
      ah = mfma32(v4u{h0.x, h0.y, h1.x, h1.y}, xb[t], ah);
    }
    *(LAS v4f*)(L + L_RED1 + w * 1024 + lane * 16) = aq;
    *(LAS v4f*)(L + L_RED1 + 8192 + w * 1024 + lane * 16) = ah;
  }
  STAMP(3);
  BAR();
#define PIN(x) asm volatile("" : "+v"(x))
  KST(0); SB(); KLD(4); SB(); KST(1); SB(); KLD(5); SB(); KST(2); SB(); KLD(6); SB(); KST(3); SB(); KLD(7); SB();
  KST(4); SB(); VLD(0); SB(); KST(5); SB(); VLD(1); SB(); KST(6); SB(); VLD(2); SB(); KST(7); SB(); VLD(3); SB();
  PIN(V[0]); VLD(4); SB(); PIN(V[1]); VLD(5); SB(); PIN(V[2]); VLD(6); SB(); PIN(V[3]); VLD(7); SB();
  v4u wor[2], w2r[2];
  {
    const u32 vo = (hh * 128 + 16 * w + (lane >> 3)) * (DM * 2) + cs * 128 + (lane & 7) * 16;
    const u32 vo2 = ((ffn2 ? hh : 0) * 128 + 16 * w + (lane >> 3)) * (DM * 2) + cs * 128 + (lane & 7) * 16;
    wor[0] = LDNT128(rwo, vo, 0); wor[1] = LDNT128(rwo, vo, 8 * DM * 2);
    w2r[0] = LDNT128(rw2, vo2, 0); w2r[1] = LDNT128(rw2, vo2, 8 * DM * 2);
  }
  SB();
  const u32 tag = *(LAS u32*)(L + L_TAG);
  STAMP(4);
  BAR();
  STAMP(5);
  float sc[8];
  {
    v4u qf[4];
#pragma unroll
    for (int i = 0; i < 4; ++i) qf[i] = *(LAS v4u*)(L + L_QL + (c >> 2) * QL_STR + i * 64 + g * 16);
    v4f s0 = {0.f, 0.f, 0.f, 0.f}, s1 = s0;
#pragma unroll
    for (int i = 0; i < 4; ++i) {
      const v4u k0 = *(LAS v4u*)(kl + c * 256 + (((4 * i + g) ^ c) * 16)), k1 = *(LAS v4u*)(kl + 4096 + c * 256 + (((4 * i + g) ^ c) * 16));
      s0 = mfma32(k0, qf[i], s0); s1 = mfma32(k1, qf[i], s1);
    }
    constexpr float SCALE = 0.08838834764831845f;
#pragma unroll
    for (int r = 0; r < 4; r += 2) {
      const u32 p0 = cvtpk(s0[r], s0[r + 1]), p1 = cvtpk(s1[r], s1[r + 1]);
      const u32 t0 = cvtpk(bflo(p0) * SCALE, bfhi(p0) * SCALE), t1 = cvtpk(bflo(p1) * SCALE, bfhi(p1) * SCALE);
      sc[r] = bflo(t0); sc[r + 1] = bfhi(t0); sc[4 + r] = bflo(t1); sc[4 + r + 1] = bfhi(t1);
    }
  }
  float mx = fmaxf(fmaxf(fmaxf(sc[0], sc[1]), fmaxf(sc[2], sc[3])), fmaxf(fmaxf(sc[4], sc[5]), fmaxf(sc[6], sc[7])));
  mx = x16_max(mx);
  LAS float* mxl = (LAS float*)(L + L_MX);
  if (g == 0 && (c & 3) == 0) mxl[(c >> 2) * 8 + w] = mx;
  BAR();
  float M;
  {
    const v4f m0 = *(LAS v4f*)(mxl + (c >> 2) * 8), m1 = *(LAS v4f*)(mxl + (c >> 2) * 8 + 4);
    M = fmaxf(fmaxf(fmaxf(m0.x, m0.y), fmaxf(m0.z, m0.w)), fmaxf(fmaxf(m1.x, m1.y), fmaxf(m1.z, m1.w)));
  }
  v4u P;
  {
    constexpr float L2E = 1.4426950408889634f;
    float e[8], ls = 0.f;
#pragma unroll
    for (int i = 0; i < 8; ++i) { e[i] = __builtin_amdgcn_exp2f((sc[i] - M) * L2E); ls += e[i]; }
    P = v4u{cvtpk(e[0], e[1]), cvtpk(e[2], e[3]), cvtpk(e[4], e[5]), cvtpk(e[6], e[7])};
    ls = x16_sum(ls);
    if (g == 0 && (c & 3) == 0) mxl[32 + (c >> 2) * 8 + w] = ls;
  }
  STAMP(6);
  {
    v4f o[8];
#pragma unroll
    for (int jj = 0; jj < 8; ++jj) {
      const u32 sel = (jj & 1) ? 0x07060302u : 0x05040100u;
      v4u bf;
#pragma unroll
      for (int pr = 0; pr < 4; ++pr) bf[pr] = __builtin_amdgcn_perm(V[2 * pr + 1][jj >> 1], V[2 * pr][jj >> 1], sel);
      o[jj] = mfma32(P, bf, v4f{0.f, 0.f, 0.f, 0.f});
    }
    LAS unsigned char* r2 = L + L_WT + w * 2048 + g * 512 + c * 32;
    *(LAS v4f*)r2 = v4f{o[0][0], o[1][0], o[2][0], o[3][0]};
    *(LAS v4f*)(r2 + 16) = v4f{o[4][0], o[5][0], o[6][0], o[7][0]};
  }
  STAMP(7);
  BAR();
  if (tid < 128) {
    const int hd = tid >> 5, dq = tid & 31;
    LAS v4f* r2 = (LAS v4f*)(L + L_WT + tid * 16);
    v4f o = r2[0];
#pragma unroll
    for (int ww = 1; ww < 8; ++ww) o += r2[ww * 128];
    const v4f l0 = *(LAS v4f*)(mxl + 32 + hd * 8), l1 = *(LAS v4f*)(mxl + 32 + hd * 8 + 4);
    const float Ls = ((l0.x + l0.y) + (l0.z + l0.w)) + ((l1.x + l1.y) + (l1.z + l1.w));
    const float inv = __builtin_amdgcn_rcpf(Ls);
    const u32 h32 = 4 * hkv + hd;
    STSC128((v4u{cvtpk(o.x * inv, o.y * inv), tag, cvtpk(o.z * inv, o.w * inv), tag}), rws, WS_PO + ((h32 * 8 + b) * 4 + s) * 512 + dq * 16, 0);
  }
  if (w == 2 && lane < 4) {
    const v4f l0 = *(LAS v4f*)(mxl + 32 + lane * 8), l1 = *(LAS v4f*)(mxl + 32 + lane * 8 + 4);
    const float Ls = ((l0.x + l0.y) + (l0.z + l0.w)) + ((l1.x + l1.y) + (l1.z + l1.w));
    const v4f m0 = *(LAS v4f*)(mxl + lane * 8), m1 = *(LAS v4f*)(mxl + lane * 8 + 4);
    const float Mh = fmaxf(fmaxf(fmaxf(m0.x, m0.y), fmaxf(m0.z, m0.w)), fmaxf(fmaxf(m1.x, m1.y), fmaxf(m1.z, m1.w)));
    STSC128((v4u{__float_as_uint(Mh), tag, __float_as_uint(Ls), tag}), rws, WS_ST + ((hkv * 8 + b) * 4 + s) * 128 + lane * 16, 0);
  }
  STAMP(8);
  {
    LAS unsigned char* wl = L + L_K + w * 8192;
    *(LAS v4u*)(wl + (lane >> 3) * 160 + (lane & 7) * 16) = wor[0]; *(LAS v4u*)(wl + (8 + (lane >> 3)) * 160 + (lane & 7) * 16) = wor[1];
    *(LAS v4u*)(wl + 2560 + (lane >> 3) * 160 + (lane & 7) * 16) = w2r[0]; *(LAS v4u*)(wl + 2560 + (8 + (lane >> 3)) * 160 + (lane & 7) * 16) = w2r[1];
    asm volatile("s_waitcnt lgkmcnt(0)" ::: "memory");
    v4u wa[4];
#pragma unroll
    for (int ct = 0; ct < 4; ++ct) {
      LAS unsigned char* tp = wl + (4 * g + (c >> 2)) * 160 + ct * 32 + (c & 3) * 8;
      const v2u t0 = trread(tp), t1 = trread(tp + 2560);
      wa[ct] = v4u{t0.x, t0.y, t1.x, t1.y};
    }
    const u32 poff = WS_PO + ((hh * 8 + w) * 4) * 512 + lane * 16;
    const u32 soff = WS_ST + ((hkv * 8 + w) * 4 + (lane >> 5)) * 128 + (hh & 3) * 16;
    const u32 hoff = WS_HG + ((ffn2 ? hh : 0) * 8 + w) * 512 + g * 128 + (c & 7) * 16;
    v4u po[2], st[2], hg = {0u, tag, 0u, tag};
    for (u32 spin = 0; spin < SPIN_CAP; ++spin) {
      asm volatile("" ::: "memory");
#pragma unroll
      for (int i = 0; i < 2; ++i) { po[i] = LDSC128(rws, poff + i * 1024, 0); st[i] = LDSC128(rws, soff + i * 256, 0); }
      if (ffn2) hg = LDSC128(rws, hoff, 0);
      bool ok = (hg.y == tag) & (hg.w == tag);
#pragma unroll
      for (int i = 0; i < 2; ++i) ok &= (po[i].y == tag) & (po[i].w == tag) & (st[i].y == tag) & (st[i].w == tag);
      if (__all(ok)) break;
    }
    STAMP(9);
    float Mg = fmaxf(__uint_as_float(st[0].x), __uint_as_float(st[1].x));
    { auto t = __builtin_amdgcn_permlane32_swap(__float_as_uint(Mg), __float_as_uint(Mg), false, false); Mg = fmaxf(__uint_as_float(t[0]), __uint_as_float(t[1])); }
    float ws_[2];
#pragma unroll
    for (int i = 0; i < 2; ++i) ws_[i] = __builtin_amdgcn_exp2f((__uint_as_float(st[i].x) - Mg) * 1.4426950408889634f) * __uint_as_float(st[i].z);
    float W = ws_[0] + ws_[1];
    { auto t = __builtin_amdgcn_permlane32_swap(__float_as_uint(W), __float_as_uint(W), false, false); W = __uint_as_float(t[0]) + __uint_as_float(t[1]); }
    const float iw = __builtin_amdgcn_rcpf(W);
    const float f0 = ws_[0] * iw, f1 = ws_[1] * iw;
    float ov[4] = {fmaf(f0, bflo(po[0].x), f1 * bflo(po[1].x)), fmaf(f0, bfhi(po[0].x), f1 * bfhi(po[1].x)),
                   fmaf(f0, bflo(po[0].z), f1 * bflo(po[1].z)), fmaf(f0, bfhi(po[0].z), f1 * bfhi(po[1].z))};
#pragma unroll
    for (int i = 0; i < 4; ++i) { auto t = __builtin_amdgcn_permlane32_swap(__float_as_uint(ov[i]), __float_as_uint(ov[i]), false, false); ov[i] = __uint_as_float(t[0]) + __uint_as_float(t[1]); }
    if (lane < 32) *(LAS v2u*)(L + L_OA + w * OA_STR + lane * 8) = v2u{cvtpk(ov[0], ov[1]), cvtpk(ov[2], ov[3])};
    BAR();
    const v2u oa = *(LAS v2u*)(L + L_OA + (c & 7) * OA_STR + (16 * w + 4 * g) * 2);
    const v4u Bf = {oa.x, oa.y, hg.x, hg.z};
    LAS unsigned char* r3 = L + L_WT + 16384 + w * 2176;
#pragma unroll
    for (int ct = 0; ct < 4; ++ct) {
      const v4f d = mfma32(wa[ct], Bf, v4f{0.f, 0.f, 0.f, 0.f});
      if (c < 8) *(LAS v4f*)(r3 + c * 272 + ct * 64 + g * 16) = d;
    }
  }
  BAR();
  if (tid < 256) {
    LAS v2f* r3 = (LAS v2f*)(L + L_WT + 16384 + (tid >> 5) * 272 + (tid & 31) * 8);
    v2f o = r3[0];
#pragma unroll
    for (int ww = 1; ww < 8; ++ww) o += r3[ww * 272];
    const int pb = tid >> 5, col = cs * 64 + (tid & 31) * 2;
    const u32 off = WS_P2 + (((pb * 32 + (col >> 4)) * 32 + hh) * 16 + (col & 15)) * 8;
    STSC128((v4u{__float_as_uint(o.x), tag, __float_as_uint(o.y), tag}), rws, off, 0);
  }
  STAMP(10);
  if (w == 0) {
    const int fb = bid & 7, c16 = bid >> 3;
    const u32 xo = fb * 1024 + c16 * 32 + (lane & 7) * 4;
    const u32 xp = __builtin_amdgcn_raw_buffer_load_b32(rx, xo, 0, 0);
    const u32 off = WS_P2 + ((fb * 32 + c16) * 32 + (lane >> 3)) * 128 + (lane & 7) * 16;
    v4u p[4];
    for (u32 spin = 0; spin < SPIN_CAP; ++spin) {
      asm volatile("" ::: "memory");
#pragma unroll
      for (int i = 0; i < 4; ++i) p[i] = LDSC128(rws, off + i * 1024, 0);
      bool ok = true;
#pragma unroll
      for (int i = 0; i < 4; ++i) ok &= (p[i].y == tag) & (p[i].w == tag);
      if (__all(ok)) break;
    }
    float s0 = (__uint_as_float(p[0].x) + __uint_as_float(p[1].x)) + (__uint_as_float(p[2].x) + __uint_as_float(p[3].x));
    float s1 = (__uint_as_float(p[0].z) + __uint_as_float(p[1].z)) + (__uint_as_float(p[2].z) + __uint_as_float(p[3].z));
    s0 += dpp<ROR8>(s0); s1 += dpp<ROR8>(s1);
    s0 = x16_sum(s0); s1 = x16_sum(s1);
    if (lane < 8) ((GAS u32*)a.out)[(fb * 512 + c16 * 16) / 2 + lane] = cvtpk(bflo(xp) + s0, bfhi(xp) + s1);
    if (bid == 0 && lane == 0) __hip_atomic_store((GAS u32*)(a.ws + WS_EPOCH), tag, __ATOMIC_RELAXED, __HIP_MEMORY_SCOPE_AGENT);
  }
  STAMP(11);
  if (bid == PROBE_BLK && w == 0) {
    u64 d = pt1 - pt0; if (d > 5000) d = 5000;
    u64 t_; asm volatile("s_memrealtime %0\n\ts_waitcnt lgkmcnt(0)" : "=s"(t_) :: "memory");
    const u64 until = t_ + 3 * d;
    for (int it = 0; it < 100000; ++it) { u64 n_; asm volatile("s_memrealtime %0\n\ts_waitcnt lgkmcnt(0)" : "=s"(n_) :: "memory"); if (n_ >= until) break; }
  }
}
}

extern "C" void kernel_launch(void* const* d_in, const int* in_sizes, int n_in, void* d_out, int out_size, void* d_ws, size_t ws_size, hipStream_t stream) {
  using namespace fused;
  Args a;
  a.x = (const u16*)d_in[0]; a.kc = (const u16*)d_in[1]; a.vc = (const u16*)d_in[2]; a.wq = (const u16*)d_in[3]; a.wo = (const u16*)d_in[4];
  a.w1 = (const u16*)d_in[6]; a.w2 = (const u16*)d_in[7]; a.out = (u16*)d_out; a.ws = (unsigned char*)d_ws;
  hipLaunchKernelGGL(fused_decode, dim3(256), dim3(NTHR), 0, stream, a);
}
```

```cpp
#include <hip/hip_runtime.h>
#include <hip/hip_bf16.h>

namespace fused {
typedef unsigned u32; typedef unsigned long long u64; typedef unsigned short u16;
typedef u32 v4u __attribute__((ext_vector_type(4)));
typedef u32 v2u __attribute__((ext_vector_type(2)));
typedef float v4f __attribute__((ext_vector_type(4)));
typedef float v2f __attribute__((ext_vector_type(2)));
typedef __bf16 bf8 __attribute__((ext_vector_type(8)));
typedef short s4 __attribute__((ext_vector_type(4)));
#define LAS __attribute__((address_space(3)))
#define GAS __attribute__((address_space(1)))

constexpr int DM = 512, SK = 4096, DH = 128, WINK = 1024, FF = 1792, NQ = 4096;
constexpr int NWAVE = 9, NTHR = NWAVE * 64;
constexpr int AUX_NT = 2, AUX_SC1 = 16;
constexpr u32 RSRC3 = 0x00020000u;
constexpr u32 SPIN_CAP = 1u << 16;

constexpr u32 WS_EPOCH = 0;
constexpr u32 WS_QG = 0x1000;
constexpr u32 WS_HG = 0x41000;
constexpr u32 WS_ST = 0x50000;
constexpr u32 WS_PO = 0x60000;
constexpr u32 WS_P2 = 0xE0000;
constexpr u32 WS_END = 0x1E0000;
constexpr u32 WS_STAMP = 0x200000;

constexpr int L_XN = 0, XN_STR = 1040;
constexpr int L_WT = 8448;
constexpr int L_RED1 = L_WT + 32768;
constexpr int L_QL = L_RED1 + 16384, QL_STR = 320;
constexpr int L_MX = L_QL + 1280;
constexpr int L_TAG = L_MX + 256;
constexpr int L_OA = L_TAG + 16, OA_STR = 272;
constexpr int L_K = 61440;
constexpr int L_BYTES = L_K + 65536;
static_assert(L_OA + 8 * OA_STR + 96 <= L_K, "LDS map");

typedef __bf16 bf2 __attribute__((ext_vector_type(2)));
__device__ __forceinline__ u32 cvtpk(float lo, float hi) { return __builtin_bit_cast(u32, __builtin_convertvector((v2f{lo, hi}), bf2)); }
__device__ __forceinline__ float bflo(u32 p) { return __builtin_bit_cast(float, p << 16); }
__device__ __forceinline__ float bfhi(u32 p) { return __builtin_bit_cast(float, p & 0xffff0000u); }
__device__ __forceinline__ float rbf(float x) { return bflo(cvtpk(x, x)); }
template <int CTRL> __device__ __forceinline__ float dpp(float x) {
  return __builtin_bit_cast(float, __builtin_amdgcn_mov_dpp(__builtin_bit_cast(int, x), CTRL, 0xf, 0xf, true));
}
constexpr int XOR1 = 0xB1, XOR2 = 0x4E, HMIR = 0x141, RMIR = 0x140, ROR8 = 0x128;
__device__ __forceinline__ float x16_sum(float x) {
  auto s = __builtin_amdgcn_permlane16_swap(__float_as_uint(x), __float_as_uint(x), false, false);
  x = __uint_as_float(s[0]) + __uint_as_float(s[1]);
  auto t = __builtin_amdgcn_permlane32_swap(__float_as_uint(x), __float_as_uint(x), false, false);
  return __uint_as_float(t[0]) + __uint_as_float(t[1]);
}
__device__ __forceinline__ float x16_max(float x) {
  auto s = __builtin_amdgcn_permlane16_swap(__float_as_uint(x), __float_as_uint(x), false, false);
  x = fmaxf(__uint_as_float(s[0]), __uint_as_float(s[1]));
  auto t = __builtin_amdgcn_permlane32_swap(__float_as_uint(x), __float_as_uint(x), false, false);
  return fmaxf(__uint_as_float(t[0]), __uint_as_float(t[1]));
}
__device__ __forceinline__ float wave_sum(float x) {
  x += dpp<XOR1>(x); x += dpp<XOR2>(x); x += dpp<HMIR>(x); x += dpp<RMIR>(x); return x16_sum(x);
}
__device__ __forceinline__ v4f mfma32(v4u a, v4u b, v4f c) {
  return __builtin_amdgcn_mfma_f32_16x16x32_bf16(__builtin_bit_cast(bf8, a), __builtin_bit_cast(bf8, b), c, 0, 0, 0);
}
__device__ __forceinline__ v2u trread(LAS unsigned char* p) {
  typedef short v4s __attribute__((ext_vector_type(4)));
  return __builtin_bit_cast(v2u, __builtin_amdgcn_ds_read_tr16_b64_v4i16((LAS v4s*)p));
}
#define BAR() do { asm volatile("s_waitcnt lgkmcnt(0)" ::: "memory"); __builtin_amdgcn_s_barrier(); asm volatile("" ::: "memory"); } while (0)
#define SB() __builtin_amdgcn_sched_barrier(0)
__device__ __forceinline__ __amdgpu_buffer_rsrc_t mkrs(const void* p, u32 bytes) { return __builtin_amdgcn_make_buffer_rsrc((void*)p, 0, (int)bytes, RSRC3); }
__device__ __forceinline__ v4u ld128(__amdgpu_buffer_rsrc_t r, u32 vo, u32 so, int aux_is_nt) { return __builtin_amdgcn_raw_buffer_load_b128(r, vo, so, AUX_NT); }
#define LDNT128(r, vo, so) __builtin_amdgcn_raw_buffer_load_b128((r), (vo), (so), AUX_NT)
#define LDNT64(r, vo, so) __builtin_amdgcn_raw_buffer_load_b64((r), (vo), (so), AUX_NT)
#define LDSC128(r, vo, so) __builtin_amdgcn_raw_buffer_load_b128((r), (vo), (so), AUX_SC1)
#define STSC128(v, r, vo, so) __builtin_amdgcn_raw_buffer_store_b128((v), (r), (vo), (so), AUX_SC1)

#define STAMP(i) do { } while (0)

struct Args { const u16* x; const u16* kc; const u16* vc; const u16* wq; const u16* wo; const u16* w1; const u16* w2; u16* out; unsigned char* ws; };

__global__ void __launch_bounds__(NTHR, 3) fused_decode(Args a) {
  __shared__ __attribute__((aligned(16))) unsigned char smem[L_BYTES];
  LAS unsigned char* L = (LAS unsigned char*)smem;
  STAMP(0);
  const int tid = threadIdx.x, lane = tid & 63, w = __builtin_amdgcn_readfirstlane(tid >> 6);
  const int bid = blockIdx.x, hkv = bid & 7, j = bid >> 3;
  const int g = lane >> 4, c = lane & 15;
  const int hh = 4 * hkv + (j & 3), cs = j >> 2;
  const bool ffn2 = hh < 14, ffn1 = (hh >= 14) && (hh < 28);
  asm volatile("" :: "s"(a.x), "s"(a.kc), "s"(a.vc), "s"(a.wq), "s"(a.wo), "s"(a.w1), "s"(a.w2), "s"(a.out), "s"(a.ws));
  const __amdgpu_buffer_rsrc_t rws = mkrs(a.ws, WS_END);
  const __amdgpu_buffer_rsrc_t rx = mkrs(a.x, 8192);
  const v4u xr = __builtin_amdgcn_raw_buffer_load_b128(mkrs(w == 8 ? (const void*)a.ws : (const void*)a.x, 8192), w == 8 ? WS_EPOCH : w * 1024 + lane * 16, 0, AUX_SC1);
  STAMP(1);
  SB(); __builtin_amdgcn_s_barrier(); SB();
  v4u wqr[2], w1r[2];
  {
    const __amdgpu_buffer_rsrc_t rwq = mkrs(a.wq, w < 8 ? DM * NQ * 2 : 0);
    const __amdgpu_buffer_rsrc_t rw1 = mkrs(a.w1, (ffn1 && w < 8) ? DM * FF * 2 : 0);
    const u32 vo = (64 * w + (lane >> 1)) * (NQ * 2) + (hkv * 512 + j * 16) * 2 + (lane & 1) * 16;
    wqr[0] = LDNT128(rwq, vo, 0); wqr[1] = LDNT128(rwq, vo, 32 * NQ * 2);
    const int t1 = ffn1 ? (hh - 14) * 8 + cs : 0;
    const u32 vo1 = (64 * w + (lane >> 1)) * (FF * 2) + t1 * 32 + (lane & 1) * 16;
    w1r[0] = LDNT128(rw1, vo1, 0); w1r[1] = LDNT128(rw1, vo1, 32 * FF * 2);
  }
  SB(); __builtin_amdgcn_s_barrier(); SB();

  if (w == 8) {
    const u32 tag = __builtin_amdgcn_readfirstlane(xr.x) + 1u;
    if (lane == 0) *(LAS u32*)(L + L_TAG) = tag;
    BAR();
    BAR();
    STAMP(2);
    {
      const int isH = lane >> 5;
      const int pb = isH ? (lane & 7) : (lane >> 2), pg = isH ? ((lane >> 3) & 3) : (lane & 3);
      LAS v4f* red = (LAS v4f*)(L + L_RED1 + isH * 8192) + (pg * 16 + pb);
      v4f s = red[0];
#pragma unroll
      for (int ww = 1; ww < 8; ++ww) s += red[ww * 64];
      v4u o;
      if (!isH) {
        o.x = cvtpk(s.x, s.y); o.z = cvtpk(s.z, s.w); o.y = tag; o.w = tag;
        STSC128(o, rws, WS_QG + ((pb * 8 + hkv) * 32 + j) * 128 + pg * 16, 0);
      } else if (ffn1) {
        float hv[4];
#pragma unroll
        for (int r = 0; r < 4; ++r) { const float hb = rbf(s[r]); hv[r] = hb * __builtin_amdgcn_rcpf(1.0f + __builtin_amdgcn_exp2f(-1.4426950408889634f * hb)); }
        o.x = cvtpk(hv[0], hv[1]); o.z = cvtpk(hv[2], hv[3]); o.y = tag; o.w = tag;
        const int t1 = (hh - 14) * 8 + cs;
        STSC128(o, rws, WS_HG + t1 * 512 + (lane - 32) * 16, 0);
      }
    }
    STAMP(3);
    {
      const int b = j >> 2;
      const u32 off = WS_QG + ((b * 8 + hkv) * 32 + (lane >> 2)) * 128 + (lane & 3) * 16;
      v4u q0, q1;
      for (u32 spin = 0; spin < SPIN_CAP; ++spin) {
        asm volatile("" ::: "memory");
        q0 = LDSC128(rws, off, 0); q1 = LDSC128(rws, off + 16 * 128, 0);
        const bool ok = (q0.y == tag) & (q0.w == tag) & (q1.y == tag) & (q1.w == tag);
        if (__all(ok)) break;
      }
      LAS unsigned char* ql = L + L_QL + (lane >> 5) * QL_STR + ((lane >> 2) & 7) * 32 + (lane & 3) * 8;
      *(LAS v2u*)ql = v2u{q0.x, q0.z};
      *(LAS v2u*)(ql + 2 * QL_STR) = v2u{q1.x, q1.z};
    }
    STAMP(4);
    BAR();
    return;
  }

  const int b = j >> 2, s = j & 3;
  const size_t kvbase = ((size_t)(b * 8 + hkv) * SK + (SK - WINK) + s * 256 + w * 32) * DH * 2;
  const __amdgpu_buffer_rsrc_t rk = mkrs((const unsigned char*)a.kc + kvbase, 8192);
  const __amdgpu_buffer_rsrc_t rv = mkrs((const unsigned char*)a.vc + kvbase, 8192);
  const __amdgpu_buffer_rsrc_t rwo = mkrs(a.wo, NQ * DM * 2);
  const __amdgpu_buffer_rsrc_t rw2 = mkrs(a.w2, ffn2 ? FF * DM * 2 : 0);
  v4u K[4], V[8];
  const u32 vvo = g * 1024 + c * 16;
  const u32 kvo = g * 256 + c * 16;
  LAS unsigned char* kl = L + L_K + w * 8192;
#define KLD(u) K[(u) & 3] = LDNT128(rk, kvo + ((u) & 3) * 1024, ((u) >> 2) * 4096)
#define KST(u) *(LAS v4u*)(kl + (4 * (u) + g) * 256 + ((c ^ ((4 * (u) + g) & 15)) * 16)) = K[(u) & 3]
#define VLD(i) V[i] = LDNT128(rv, vvo + ((i) & 3) * 256, ((i) >> 2) * 4096)
  KLD(0); KLD(1); KLD(2); KLD(3);
  SB();
  {
    float xv[8];
#pragma unroll
    for (int i = 0; i < 4; ++i) { xv[2 * i] = bflo(xr[i]); xv[2 * i + 1] = bfhi(xr[i]); }
    float sm = ((xv[0] + xv[1]) + (xv[2] + xv[3])) + ((xv[4] + xv[5]) + (xv[6] + xv[7]));
    sm = wave_sum(sm);
    const float mean = sm * (1.0f / DM);
    float ss = 0.f;
#pragma unroll
    for (int i = 0; i < 8; ++i) { const float d = xv[i] - mean; ss = fmaf(d, d, ss); }
    ss = wave_sum(ss);
    const float mb = rbf(mean), var = rbf(ss * (1.0f / DM));
    const float den = rbf(__builtin_amdgcn_sqrtf(rbf(var + 1e-5f)));
    const float inv = __builtin_amdgcn_rcpf(den);
    v4u o;
#pragma unroll
    for (int i = 0; i < 4; ++i) {
      const u32 dd = cvtpk(xv[2 * i] - mb, xv[2 * i + 1] - mb);
      o[i] = cvtpk(bflo(dd) * inv, bfhi(dd) * inv);
    }
    *(LAS v4u*)(L + L_XN + w * XN_STR + lane * 16) = o;
  }
  STAMP(2);
  BAR();
  {
    LAS unsigned char* wt = L + L_WT + w * 2048;
    *(LAS v4u*)(wt + lane * 16) = wqr[0]; *(LAS v4u*)(wt + 1024 + lane * 16) = wqr[1];
    *(LAS v4u*)(wt + 16384 + lane * 16) = w1r[0]; *(LAS v4u*)(wt + 16384 + 1024 + lane * 16) = w1r[1];
    asm volatile("s_waitcnt lgkmcnt(0)" ::: "memory");
    v4u xb[2];
#pragma unroll
    for (int t = 0; t < 2; ++t) {
      LAS unsigned char* xp = L + L_XN + (c & 7) * XN_STR + (64 * w + 32 * t + 4 * g) * 2;
      const v2u lo = *(LAS v2u*)xp, hi = *(LAS v2u*)(xp + 32);
      xb[t] = v4u{lo.x, lo.y, hi.x, hi.y};
    }
    v4f aq = {0.f, 0.f, 0.f, 0.f}, ah = {0.f, 0.f, 0.f, 0.f};
#pragma unroll
    for (int t = 0; t < 2; ++t) {
      const v2u q0 = trread(wt + t * 1024 + lane * 8), q1 = trread(wt + t * 1024 + 512 + lane * 8);
      aq = mfma32(v4u{q0.x, q0.y, q1.x, q1.y}, xb[t], aq);
      const v2u h0 = trread(wt + 16384 + t * 1024 + lane * 8), h1 = trread(wt + 16384 + t * 1024 + 512 + lane * 8);
      ah = mfma32(v4u{h0.x, h0.y, h1.x, h1.y}, xb[t], ah);
    }
    *(LAS v4f*)(L + L_RED1 + w * 1024 + lane * 16) = aq;
    *(LAS v4f*)(L + L_RED1 + 8192 + w * 1024 + lane * 16) = ah;
  }
  STAMP(3);
  BAR();
#define PIN(x) asm volatile("" : "+v"(x))
  KST(0); SB(); KLD(4); SB(); KST(1); SB(); KLD(5); SB(); KST(2); SB(); KLD(6); SB(); KST(3); SB(); KLD(7); SB();
  KST(4); SB(); VLD(0); SB(); KST(5); SB(); VLD(1); SB(); KST(6); SB(); VLD(2); SB(); KST(7); SB(); VLD(3); SB();
  PIN(V[0]); VLD(4); SB(); PIN(V[1]); VLD(5); SB(); PIN(V[2]); VLD(6); SB(); PIN(V[3]); VLD(7); SB();
  v4u wor[2], w2r[2];
  {
    const u32 vo = (hh * 128 + 16 * w + (lane >> 3)) * (DM * 2) + cs * 128 + (lane & 7) * 16;
    const u32 vo2 = ((ffn2 ? hh : 0) * 128 + 16 * w + (lane >> 3)) * (DM * 2) + cs * 128 + (lane & 7) * 16;
    wor[0] = LDNT128(rwo, vo, 0); wor[1] = LDNT128(rwo, vo, 8 * DM * 2);
    w2r[0] = LDNT128(rw2, vo2, 0); w2r[1] = LDNT128(rw2, vo2, 8 * DM * 2);
  }
  SB();
  const u32 tag = *(LAS u32*)(L + L_TAG);
  STAMP(4);
  BAR();
  STAMP(5);
  float sc[8];
  {
    v4u qf[4];
#pragma unroll
    for (int i = 0; i < 4; ++i) qf[i] = *(LAS v4u*)(L + L_QL + (c >> 2) * QL_STR + i * 64 + g * 16);
    v4f s0 = {0.f, 0.f, 0.f, 0.f}, s1 = s0;
#pragma unroll
    for (int i = 0; i < 4; ++i) {
      const v4u k0 = *(LAS v4u*)(kl + c * 256 + (((4 * i + g) ^ c) * 16)), k1 = *(LAS v4u*)(kl + 4096 + c * 256 + (((4 * i + g) ^ c) * 16));
      s0 = mfma32(k0, qf[i], s0); s1 = mfma32(k1, qf[i], s1);
    }
    constexpr float SCALE = 0.08838834764831845f;
#pragma unroll
    for (int r = 0; r < 4; r += 2) {
      const u32 p0 = cvtpk(s0[r], s0[r + 1]), p1 = cvtpk(s1[r], s1[r + 1]);
      const u32 t0 = cvtpk(bflo(p0) * SCALE, bfhi(p0) * SCALE), t1 = cvtpk(bflo(p1) * SCALE, bfhi(p1) * SCALE);
      sc[r] = bflo(t0); sc[r + 1] = bfhi(t0); sc[4 + r] = bflo(t1); sc[4 + r + 1] = bfhi(t1);
    }
  }
  float mx = fmaxf(fmaxf(fmaxf(sc[0], sc[1]), fmaxf(sc[2], sc[3])), fmaxf(fmaxf(sc[4], sc[5]), fmaxf(sc[6], sc[7])));
  mx = x16_max(mx);
  LAS float* mxl = (LAS float*)(L + L_MX);
  if (g == 0 && (c & 3) == 0) mxl[(c >> 2) * 8 + w] = mx;
  BAR();
  float M;
  {
    const v4f m0 = *(LAS v4f*)(mxl + (c >> 2) * 8), m1 = *(LAS v4f*)(mxl + (c >> 2) * 8 + 4);
    M = fmaxf(fmaxf(fmaxf(m0.x, m0.y), fmaxf(m0.z, m0.w)), fmaxf(fmaxf(m1.x, m1.y), fmaxf(m1.z, m1.w)));
  }
  v4u P;
  {
    constexpr float L2E = 1.4426950408889634f;
    float e[8], ls = 0.f;
#pragma unroll
    for (int i = 0; i < 8; ++i) { e[i] = __builtin_amdgcn_exp2f((sc[i] - M) * L2E); ls += e[i]; }
    P = v4u{cvtpk(e[0], e[1]), cvtpk(e[2], e[3]), cvtpk(e[4], e[5]), cvtpk(e[6], e[7])};
    ls = x16_sum(ls);
    if (g == 0 && (c & 3) == 0) mxl[32 + (c >> 2) * 8 + w] = ls;
  }
  STAMP(6);
  {
    v4f o[8];
#pragma unroll
    for (int jj = 0; jj < 8; ++jj) {
      const u32 sel = (jj & 1) ? 0x07060302u : 0x05040100u;
      v4u bf;
#pragma unroll
      for (int pr = 0; pr < 4; ++pr) bf[pr] = __builtin_amdgcn_perm(V[2 * pr + 1][jj >> 1], V[2 * pr][jj >> 1], sel);
      o[jj] = mfma32(P, bf, v4f{0.f, 0.f, 0.f, 0.f});
    }
    LAS unsigned char* r2 = L + L_WT + w * 2048 + g * 512 + c * 32;
    *(LAS v4f*)r2 = v4f{o[0][0], o[1][0], o[2][0], o[3][0]};
    *(LAS v4f*)(r2 + 16) = v4f{o[4][0], o[5][0], o[6][0], o[7][0]};
  }
  STAMP(7);
  BAR();
  if (tid < 128) {
    const int hd = tid >> 5, dq = tid & 31;
    LAS v4f* r2 = (LAS v4f*)(L + L_WT + tid * 16);
    v4f o = r2[0];
#pragma unroll
    for (int ww = 1; ww < 8; ++ww) o += r2[ww * 128];
    const v4f l0 = *(LAS v4f*)(mxl + 32 + hd * 8), l1 = *(LAS v4f*)(mxl + 32 + hd * 8 + 4);
    const float Ls = ((l0.x + l0.y) + (l0.z + l0.w)) + ((l1.x + l1.y) + (l1.z + l1.w));
    const float inv = __builtin_amdgcn_rcpf(Ls);
    const u32 h32 = 4 * hkv + hd;
    STSC128((v4u{cvtpk(o.x * inv, o.y * inv), tag, cvtpk(o.z * inv, o.w * inv), tag}), rws, WS_PO + ((h32 * 8 + b) * 4 + s) * 512 + dq * 16, 0);
  }
  if (w == 2 && lane < 4) {
    const v4f l0 = *(LAS v4f*)(mxl + 32 + lane * 8), l1 = *(LAS v4f*)(mxl + 32 + lane * 8 + 4);
    const float Ls = ((l0.x + l0.y) + (l0.z + l0.w)) + ((l1.x + l1.y) + (l1.z + l1.w));
    const v4f m0 = *(LAS v4f*)(mxl + lane * 8), m1 = *(LAS v4f*)(mxl + lane * 8 + 4);
    const float Mh = fmaxf(fmaxf(fmaxf(m0.x, m0.y), fmaxf(m0.z, m0.w)), fmaxf(fmaxf(m1.x, m1.y), fmaxf(m1.z, m1.w)));
    STSC128((v4u{__float_as_uint(Mh), tag, __float_as_uint(Ls), tag}), rws, WS_ST + ((hkv * 8 + b) * 4 + s) * 128 + lane * 16, 0);
  }
  STAMP(8);
  {
    LAS unsigned char* wl = L + L_K + w * 8192;
    *(LAS v4u*)(wl + (lane >> 3) * 160 + (lane & 7) * 16) = wor[0]; *(LAS v4u*)(wl + (8 + (lane >> 3)) * 160 + (lane & 7) * 16) = wor[1];
    *(LAS v4u*)(wl + 2560 + (lane >> 3) * 160 + (lane & 7) * 16) = w2r[0]; *(LAS v4u*)(wl + 2560 + (8 + (lane >> 3)) * 160 + (lane & 7) * 16) = w2r[1];
    asm volatile("s_waitcnt lgkmcnt(0)" ::: "memory");
    v4u wa[4];
#pragma unroll
    for (int ct = 0; ct < 4; ++ct) {
      LAS unsigned char* tp = wl + (4 * g + (c >> 2)) * 160 + ct * 32 + (c & 3) * 8;
      const v2u t0 = trread(tp), t1 = trread(tp + 2560);
      wa[ct] = v4u{t0.x, t0.y, t1.x, t1.y};
    }
    const u32 poff = WS_PO + ((hh * 8 + w) * 4) * 512 + lane * 16;
    const u32 soff = WS_ST + ((hkv * 8 + w) * 4 + (lane >> 5)) * 128 + (hh & 3) * 16;
    const u32 hoff = WS_HG + ((ffn2 ? hh : 0) * 8 + w) * 512 + g * 128 + (c & 7) * 16;
    v4u po[2], st[2], hg = {0u, tag, 0u, tag};
    for (u32 spin = 0; spin < SPIN_CAP; ++spin) {
      asm volatile("" ::: "memory");
#pragma unroll
      for (int i = 0; i < 2; ++i) { po[i] = LDSC128(rws, poff + i * 1024, 0); st[i] = LDSC128(rws, soff + i * 256, 0); }
      if (ffn2) hg = LDSC128(rws, hoff, 0);
      bool ok = (hg.y == tag) & (hg.w == tag);
#pragma unroll
      for (int i = 0; i < 2; ++i) ok &= (po[i].y == tag) & (po[i].w == tag) & (st[i].y == tag) & (st[i].w == tag);
      if (__all(ok)) break;
    }
    STAMP(9);
    float Mg = fmaxf(__uint_as_float(st[0].x), __uint_as_float(st[1].x));
    { auto t = __builtin_amdgcn_permlane32_swap(__float_as_uint(Mg), __float_as_uint(Mg), false, false); Mg = fmaxf(__uint_as_float(t[0]), __uint_as_float(t[1])); }
    float ws_[2];
#pragma unroll
    for (int i = 0; i < 2; ++i) ws_[i] = __builtin_amdgcn_exp2f((__uint_as_float(st[i].x) - Mg) * 1.4426950408889634f) * __uint_as_float(st[i].z);
    float W = ws_[0] + ws_[1];
    { auto t = __builtin_amdgcn_permlane32_swap(__float_as_uint(W), __float_as_uint(W), false, false); W = __uint_as_float(t[0]) + __uint_as_float(t[1]); }
    const float iw = __builtin_amdgcn_rcpf(W);
    const float f0 = ws_[0] * iw, f1 = ws_[1] * iw;
    float ov[4] = {fmaf(f0, bflo(po[0].x), f1 * bflo(po[1].x)), fmaf(f0, bfhi(po[0].x), f1 * bfhi(po[1].x)),
                   fmaf(f0, bflo(po[0].z), f1 * bflo(po[1].z)), fmaf(f0, bfhi(po[0].z), f1 * bfhi(po[1].z))};
#pragma unroll
    for (int i = 0; i < 4; ++i) { auto t = __builtin_amdgcn_permlane32_swap(__float_as_uint(ov[i]), __float_as_uint(ov[i]), false, false); ov[i] = __uint_as_float(t[0]) + __uint_as_float(t[1]); }
    if (lane < 32) *(LAS v2u*)(L + L_OA + w * OA_STR + lane * 8) = v2u{cvtpk(ov[0], ov[1]), cvtpk(ov[2], ov[3])};
    BAR();
    const v2u oa = *(LAS v2u*)(L + L_OA + (c & 7) * OA_STR + (16 * w + 4 * g) * 2);
    const v4u Bf = {oa.x, oa.y, hg.x, hg.z};
    LAS unsigned char* r3 = L + L_WT + 16384 + w * 2176;
#pragma unroll
    for (int ct = 0; ct < 4; ++ct) {
      const v4f d = mfma32(wa[ct], Bf, v4f{0.f, 0.f, 0.f, 0.f});
      if (c < 8) *(LAS v4f*)(r3 + c * 272 + ct * 64 + g * 16) = d;
    }
  }
  BAR();
  if (tid < 256) {
    LAS v2f* r3 = (LAS v2f*)(L + L_WT + 16384 + (tid >> 5) * 272 + (tid & 31) * 8);
    v2f o = r3[0];
#pragma unroll
    for (int ww = 1; ww < 8; ++ww) o += r3[ww * 272];
    const int pb = tid >> 5, col = cs * 64 + (tid & 31) * 2;
    const u32 off = WS_P2 + (((pb * 32 + (col >> 4)) * 32 + hh) * 16 + (col & 15)) * 8;
    STSC128((v4u{__float_as_uint(o.x), tag, __float_as_uint(o.y), tag}), rws, off, 0);
  }
  STAMP(10);
  if (w == 0) {
    const int fb = bid & 7, c16 = bid >> 3;
    const u32 xo = fb * 1024 + c16 * 32 + (lane & 7) * 4;
    const u32 xp = __builtin_amdgcn_raw_buffer_load_b32(rx, xo, 0, 0);
    const u32 off = WS_P2 + ((fb * 32 + c16) * 32 + (lane >> 3)) * 128 + (lane & 7) * 16;
    v4u p[4];
    for (u32 spin = 0; spin < SPIN_CAP; ++spin) {
      asm volatile("" ::: "memory");
#pragma unroll
      for (int i = 0; i < 4; ++i) p[i] = LDSC128(rws, off + i * 1024, 0);
      bool ok = true;
#pragma unroll
      for (int i = 0; i < 4; ++i) ok &= (p[i].y == tag) & (p[i].w == tag);
      if (__all(ok)) break;
    }
    float s0 = (__uint_as_float(p[0].x) + __uint_as_float(p[1].x)) + (__uint_as_float(p[2].x) + __uint_as_float(p[3].x));
    float s1 = (__uint_as_float(p[0].z) + __uint_as_float(p[1].z)) + (__uint_as_float(p[2].z) + __uint_as_float(p[3].z));
    s0 += dpp<ROR8>(s0); s1 += dpp<ROR8>(s1);
    s0 = x16_sum(s0); s1 = x16_sum(s1);
    if (lane < 8) ((GAS u32*)a.out)[(fb * 512 + c16 * 16) / 2 + lane] = cvtpk(bflo(xp) + s0, bfhi(xp) + s1);
    if (bid == 0 && lane == 0) __hip_atomic_store((GAS u32*)(a.ws + WS_EPOCH), tag, __ATOMIC_RELAXED, __HIP_MEMORY_SCOPE_AGENT);
  }
  STAMP(11);
  if (bid < 8 && w == 0) asm volatile("buffer_wbl2 sc1\n\ts_waitcnt vmcnt(0)" ::: "memory");
}
}

extern "C" void kernel_launch(void* const* d_in, const int* in_sizes, int n_in, void* d_out, int out_size, void* d_ws, size_t ws_size, hipStream_t stream) {
  using namespace fused;
  Args a;
  a.x = (const u16*)d_in[0]; a.kc = (const u16*)d_in[1]; a.vc = (const u16*)d_in[2]; a.wq = (const u16*)d_in[3]; a.wo = (const u16*)d_in[4];
  a.w1 = (const u16*)d_in[6]; a.w2 = (const u16*)d_in[7]; a.out = (u16*)d_out; a.ws = (unsigned char*)d_ws;
  hipLaunchKernelGGL(fused_decode, dim3(256), dim3(NTHR), 0, stream, a);
}
```

```cpp
#include <hip/hip_runtime.h>
#include <hip/hip_bf16.h>

namespace fused {
typedef unsigned u32; typedef unsigned long long u64; typedef unsigned short u16;
typedef u32 v4u __attribute__((ext_vector_type(4)));
typedef u32 v2u __attribute__((ext_vector_type(2)));
typedef float v4f __attribute__((ext_vector_type(4)));
typedef float v2f __attribute__((ext_vector_type(2)));
typedef __bf16 bf8 __attribute__((ext_vector_type(8)));
typedef short s4 __attribute__((ext_vector_type(4)));
#define LAS __attribute__((address_space(3)))
#define GAS __attribute__((address_space(1)))

constexpr int DM = 512, SK = 4096, DH = 128, WINK = 1024, FF = 1792, NQ = 4096;
constexpr int NWAVE = 9, NTHR = NWAVE * 64;
constexpr int AUX_NT = 2, AUX_SC1 = 16;
constexpr u32 RSRC3 = 0x00020000u;
constexpr u32 SPIN_CAP = 1u << 16;

constexpr u32 WS_EPOCH = 0;
constexpr u32 WS_QG = 0x1000;
constexpr u32 WS_HG = 0x41000;
constexpr u32 WS_ST = 0x50000;
constexpr u32 WS_PO = 0x60000;
constexpr u32 WS_P2 = 0xE0000;
constexpr u32 WS_END = 0x1E0000;
constexpr u32 WS_STAMP = 0x200000;

constexpr int L_XN = 0, XN_STR = 1040;
constexpr int L_WT = 8448;
constexpr int L_RED1 = L_WT + 32768;
constexpr int L_QL = L_RED1 + 16384, QL_STR = 320;
constexpr int L_MX = L_QL + 1280;
constexpr int L_TAG = L_MX + 256;
constexpr int L_OA = L_TAG + 16, OA_STR = 272;
constexpr int L_K = 61440;
constexpr int L_BYTES = L_K + 65536;
static_assert(L_OA + 8 * OA_STR + 96 <= L_K, "LDS map");

typedef __bf16 bf2 __attribute__((ext_vector_type(2)));
__device__ __forceinline__ u32 cvtpk(float lo, float hi) { return __builtin_bit_cast(u32, __builtin_convertvector((v2f{lo, hi}), bf2)); }
__device__ __forceinline__ float bflo(u32 p) { return __builtin_bit_cast(float, p << 16); }
__device__ __forceinline__ float bfhi(u32 p) { return __builtin_bit_cast(float, p & 0xffff0000u); }
__device__ __forceinline__ float rbf(float x) { return bflo(cvtpk(x, x)); }
template <int CTRL> __device__ __forceinline__ float dpp(float x) {
  return __builtin_bit_cast(float, __builtin_amdgcn_mov_dpp(__builtin_bit_cast(int, x), CTRL, 0xf, 0xf, true));
}
constexpr int XOR1 = 0xB1, XOR2 = 0x4E, HMIR = 0x141, RMIR = 0x140, ROR8 = 0x128;
__device__ __forceinline__ float x16_sum(float x) {
  auto s = __builtin_amdgcn_permlane16_swap(__float_as_uint(x), __float_as_uint(x), false, false);
  x = __uint_as_float(s[0]) + __uint_as_float(s[1]);
  auto t = __builtin_amdgcn_permlane32_swap(__float_as_uint(x), __float_as_uint(x), false, false);
  return __uint_as_float(t[0]) + __uint_as_float(t[1]);
}
__device__ __forceinline__ float x16_max(float x) {
  auto s = __builtin_amdgcn_permlane16_swap(__float_as_uint(x), __float_as_uint(x), false, false);
  x = fmaxf(__uint_as_float(s[0]), __uint_as_float(s[1]));
  auto t = __builtin_amdgcn_permlane32_swap(__float_as_uint(x), __float_as_uint(x), false, false);
  return fmaxf(__uint_as_float(t[0]), __uint_as_float(t[1]));
}
__device__ __forceinline__ float wave_sum(float x) {
  x += dpp<XOR1>(x); x += dpp<XOR2>(x); x += dpp<HMIR>(x); x += dpp<RMIR>(x); return x16_sum(x);
}
__device__ __forceinline__ v4f mfma32(v4u a, v4u b, v4f c) {
  return __builtin_amdgcn_mfma_f32_16x16x32_bf16(__builtin_bit_cast(bf8, a), __builtin_bit_cast(bf8, b), c, 0, 0, 0);
}
__device__ __forceinline__ v2u trread(LAS unsigned char* p) {
  typedef short v4s __attribute__((ext_vector_type(4)));
  return __builtin_bit_cast(v2u, __builtin_amdgcn_ds_read_tr16_b64_v4i16((LAS v4s*)p));
}
#define BAR() do { asm volatile("s_waitcnt lgkmcnt(0)" ::: "memory"); __builtin_amdgcn_s_barrier(); asm volatile("" ::: "memory"); } while (0)
#define SB() __builtin_amdgcn_sched_barrier(0)
__device__ __forceinline__ __amdgpu_buffer_rsrc_t mkrs(const void* p, u32 bytes) { return __builtin_amdgcn_make_buffer_rsrc((void*)p, 0, (int)bytes, RSRC3); }
__device__ __forceinline__ v4u ld128(__amdgpu_buffer_rsrc_t r, u32 vo, u32 so, int aux_is_nt) { return __builtin_amdgcn_raw_buffer_load_b128(r, vo, so, AUX_NT); }
#define LDNT128(r, vo, so) __builtin_amdgcn_raw_buffer_load_b128((r), (vo), (so), AUX_NT)
#define LDNT64(r, vo, so) __builtin_amdgcn_raw_buffer_load_b64((r), (vo), (so), AUX_NT)
#define LDSC128(r, vo, so) __builtin_amdgcn_raw_buffer_load_b128((r), (vo), (so), AUX_SC1)
#define STSC128(v, r, vo, so) __builtin_amdgcn_raw_buffer_store_b128((v), (r), (vo), (so), AUX_SC1)

#define STAMP(i) do { } while (0)

struct Args { const u16* x; const u16* kc; const u16* vc; const u16* wq; const u16* wo; const u16* w1; const u16* w2; u16* out; unsigned char* ws; };

__global__ void __launch_bounds__(NTHR, 3) fused_decode(Args a) {
  __shared__ __attribute__((aligned(16))) unsigned char smem[L_BYTES];
  LAS unsigned char* L = (LAS unsigned char*)smem;
  STAMP(0);
  const int tid = threadIdx.x, lane = tid & 63, w = __builtin_amdgcn_readfirstlane(tid >> 6);
  const int bid = blockIdx.x, hkv = bid & 7, j = bid >> 3;
  const int g = lane >> 4, c = lane & 15;
  const int hh = 4 * hkv + (j & 3), cs = j >> 2;
  const bool ffn2 = hh < 14, ffn1 = (hh >= 14) && (hh < 28);
  asm volatile("" :: "s"(a.x), "s"(a.kc), "s"(a.vc), "s"(a.wq), "s"(a.wo), "s"(a.w1), "s"(a.w2), "s"(a.out), "s"(a.ws));
  const __amdgpu_buffer_rsrc_t rws = mkrs(a.ws, WS_END);
  const __amdgpu_buffer_rsrc_t rx = mkrs(a.x, 8192);
  const v4u xr = __builtin_amdgcn_raw_buffer_load_b128(mkrs(w == 8 ? (const void*)a.ws : (const void*)a.x, 8192), w == 8 ? WS_EPOCH : w * 1024 + lane * 16, 0, AUX_SC1);
  STAMP(1);
  SB(); __builtin_amdgcn_s_barrier(); SB();
  v4u wqr[2], w1r[2];
  {
    const __amdgpu_buffer_rsrc_t rwq = mkrs(a.wq, w < 8 ? DM * NQ * 2 : 0);
    const __amdgpu_buffer_rsrc_t rw1 = mkrs(a.w1, (ffn1 && w < 8) ? DM * FF * 2 : 0);
    const u32 vo = (64 * w + (lane >> 1)) * (NQ * 2) + (hkv * 512 + j * 16) * 2 + (lane & 1) * 16;
    wqr[0] = LDNT128(rwq, vo, 0); wqr[1] = LDNT128(rwq, vo, 32 * NQ * 2);
    const int t1 = ffn1 ? (hh - 14) * 8 + cs : 0;
    const u32 vo1 = (64 * w + (lane >> 1)) * (FF * 2) + t1 * 32 + (lane & 1) * 16;
    w1r[0] = LDNT128(rw1, vo1, 0); w1r[1] = LDNT128(rw1, vo1, 32 * FF * 2);
  }
  SB(); __builtin_amdgcn_s_barrier(); SB();

  if (w == 8) {
    const u32 tag = __builtin_amdgcn_readfirstlane(xr.x) + 1u;
    if (lane == 0) *(LAS u32*)(L + L_TAG) = tag;
    BAR();
    BAR();
    STAMP(2);
    {
      const int isH = lane >> 5;
      const int pb = isH ? (lane & 7) : (lane >> 2), pg = isH ? ((lane >> 3) & 3) : (lane & 3);
      LAS v4f* red = (LAS v4f*)(L + L_RED1 + isH * 8192) + (pg * 16 + pb);
      v4f s = red[0];
#pragma unroll
      for (int ww = 1; ww < 8; ++ww) s += red[ww * 64];
      v4u o;
      if (!isH) {
        o.x = cvtpk(s.x, s.y); o.z = cvtpk(s.z, s.w); o.y = tag; o.w = tag;
        STSC128(o, rws, WS_QG + ((pb * 8 + hkv) * 32 + j) * 128 + pg * 16, 0);
      } else if (ffn1) {
        float hv[4];
#pragma unroll
        for (int r = 0; r < 4; ++r) { const float hb = rbf(s[r]); hv[r] = hb * __builtin_amdgcn_rcpf(1.0f + __builtin_amdgcn_exp2f(-1.4426950408889634f * hb)); }
        o.x = cvtpk(hv[0], hv[1]); o.z = cvtpk(hv[2], hv[3]); o.y = tag; o.w = tag;
        const int t1 = (hh - 14) * 8 + cs;
        STSC128(o, rws, WS_HG + t1 * 512 + (lane - 32) * 16, 0);
      }
    }
    STAMP(3);
    {
      const int b = j >> 2;
      const u32 off = WS_QG + ((b * 8 + hkv) * 32 + (lane >> 2)) * 128 + (lane & 3) * 16;
      v4u q0, q1;
      for (u32 spin = 0; spin < SPIN_CAP; ++spin) {
        asm volatile("" ::: "memory");
        q0 = LDSC128(rws, off, 0); q1 = LDSC128(rws, off + 16 * 128, 0);
        const bool ok = (q0.y == tag) & (q0.w == tag) & (q1.y == tag) & (q1.w == tag);
        if (__all(ok)) break;
      }
      LAS unsigned char* ql = L + L_QL + (lane >> 5) * QL_STR + ((lane >> 2) & 7) * 32 + (lane & 3) * 8;
      *(LAS v2u*)ql = v2u{q0.x, q0.z};
      *(LAS v2u*)(ql + 2 * QL_STR) = v2u{q1.x, q1.z};
    }
    STAMP(4);
    BAR();
    return;
  }

  const int b = j >> 2, s = j & 3;
  const size_t kvbase = ((size_t)(b * 8 + hkv) * SK + (SK - WINK) + s * 256 + w * 32) * DH * 2;
  const __amdgpu_buffer_rsrc_t rk = mkrs((const unsigned char*)a.kc + kvbase, 8192);
  const __amdgpu_buffer_rsrc_t rv = mkrs((const unsigned char*)a.vc + kvbase, 8192);
  const __amdgpu_buffer_rsrc_t rwo = mkrs(a.wo, NQ * DM * 2);
  const __amdgpu_buffer_rsrc_t rw2 = mkrs(a.w2, ffn2 ? FF * DM * 2 : 0);
  v4u K[4], V[8];
  const u32 vvo = g * 1024 + c * 16;
  const u32 kvo = g * 256 + c * 16;
  LAS unsigned char* kl = L + L_K + w * 8192;
#define KLD(u) K[(u) & 3] = LDNT128(rk, kvo + ((u) & 3) * 1024, ((u) >> 2) * 4096)
#define KST(u) *(LAS v4u*)(kl + (4 * (u) + g) * 256 + ((c ^ ((4 * (u) + g) & 15)) * 16)) = K[(u) & 3]
#define VLD(i) V[i] = LDNT128(rv, vvo + ((i) & 3) * 256, ((i) >> 2) * 4096)
  KLD(0); KLD(1); KLD(2); KLD(3);
  SB();
  {
    float xv[8];
#pragma unroll
    for (int i = 0; i < 4; ++i) { xv[2 * i] = bflo(xr[i]); xv[2 * i + 1] = bfhi(xr[i]); }
    float sm = ((xv[0] + xv[1]) + (xv[2] + xv[3])) + ((xv[4] + xv[5]) + (xv[6] + xv[7]));
    sm = wave_sum(sm);
    const float mean = sm * (1.0f / DM);
    float ss = 0.f;
#pragma unroll
    for (int i = 0; i < 8; ++i) { const float d = xv[i] - mean; ss = fmaf(d, d, ss); }
    ss = wave_sum(ss);
    const float mb = rbf(mean), var = rbf(ss * (1.0f / DM));
    const float den = rbf(__builtin_amdgcn_sqrtf(rbf(var + 1e-5f)));
    const float inv = __builtin_amdgcn_rcpf(den);
    v4u o;
#pragma unroll
    for (int i = 0; i < 4; ++i) {
      const u32 dd = cvtpk(xv[2 * i] - mb, xv[2 * i + 1] - mb);
      o[i] = cvtpk(bflo(dd) * inv, bfhi(dd) * inv);
    }
    *(LAS v4u*)(L + L_XN + w * XN_STR + lane * 16) = o;
  }
  STAMP(2);
  BAR();
  {
    LAS unsigned char* wt = L + L_WT + w * 2048;
    *(LAS v4u*)(wt + lane * 16) = wqr[0]; *(LAS v4u*)(wt + 1024 + lane * 16) = wqr[1];
    *(LAS v4u*)(wt + 16384 + lane * 16) = w1r[0]; *(LAS v4u*)(wt + 16384 + 1024 + lane * 16) = w1r[1];
    asm volatile("s_waitcnt lgkmcnt(0)" ::: "memory");
    v4u xb[2];
#pragma unroll
    for (int t = 0; t < 2; ++t) {
      LAS unsigned char* xp = L + L_XN + (c & 7) * XN_STR + (64 * w + 32 * t + 4 * g) * 2;
      const v2u lo = *(LAS v2u*)xp, hi = *(LAS v2u*)(xp + 32);
      xb[t] = v4u{lo.x, lo.y, hi.x, hi.y};
    }
    v4f aq = {0.f, 0.f, 0.f, 0.f}, ah = {0.f, 0.f, 0.f, 0.f};
#pragma unroll
    for (int t = 0; t < 2; ++t) {
      const v2u q0 = trread(wt + t * 1024 + lane * 8), q1 = trread(wt + t * 1024 + 512 + lane * 8);
      aq = mfma32(v4u{q0.x, q0.y, q1.x, q1.y}, xb[t], aq);
      const v2u h0 = trread(wt + 16384 + t * 1024 + lane * 8), h1 = trread(wt + 16384 + t * 1024 + 512 + lane * 8);
      ah = mfma32(v4u{h0.x, h0.y, h1.x, h1.y}, xb[t], ah);
    }
    *(LAS v4f*)(L + L_RED1 + w * 1024 + lane * 16) = aq;
    *(LAS v4f*)(L + L_RED1 + 8192 + w * 1024 + lane * 16) = ah;
  }
  STAMP(3);
  BAR();
#define PIN(x) asm volatile("" : "+v"(x))
  KST(0); SB(); KLD(4); SB(); KST(1); SB(); KLD(5); SB(); KST(2); SB(); KLD(6); SB(); KST(3); SB(); KLD(7); SB();
  KST(4); SB(); VLD(0); SB(); KST(5); SB(); VLD(1); SB(); KST(6); SB(); VLD(2); SB(); KST(7); SB(); VLD(3); SB();
  PIN(V[0]); VLD(4); SB(); PIN(V[1]); VLD(5); SB(); PIN(V[2]); VLD(6); SB(); PIN(V[3]); VLD(7); SB();
  v4u wor[2], w2r[2];
  {
    const u32 vo = (hh * 128 + 16 * w + (lane >> 3)) * (DM * 2) + cs * 128 + (lane & 7) * 16;
    const u32 vo2 = ((ffn2 ? hh : 0) * 128 + 16 * w + (lane >> 3)) * (DM * 2) + cs * 128 + (lane & 7) * 16;
    wor[0] = LDNT128(rwo, vo, 0); wor[1] = LDNT128(rwo, vo, 8 * DM * 2);
    w2r[0] = LDNT128(rw2, vo2, 0); w2r[1] = LDNT128(rw2, vo2, 8 * DM * 2);
  }
  SB();
  const u32 tag = *(LAS u32*)(L + L_TAG);
  STAMP(4);
  BAR();
  STAMP(5);
  float sc[8];
  {
    v4u qf[4];
#pragma unroll
    for (int i = 0; i < 4; ++i) qf[i] = *(LAS v4u*)(L + L_QL + (c >> 2) * QL_STR + i * 64 + g * 16);
    v4f s0 = {0.f, 0.f, 0.f, 0.f}, s1 = s0;
#pragma unroll
    for (int i = 0; i < 4; ++i) {
      const v4u k0 = *(LAS v4u*)(kl + c * 256 + (((4 * i + g) ^ c) * 16)), k1 = *(LAS v4u*)(kl + 4096 + c * 256 + (((4 * i + g) ^ c) * 16));
      s0 = mfma32(k0, qf[i], s0); s1 = mfma32(k1, qf[i], s1);
    }
    constexpr float SCALE = 0.08838834764831845f;
#pragma unroll
    for (int r = 0; r < 4; r += 2) {
      const u32 p0 = cvtpk(s0[r], s0[r + 1]), p1 = cvtpk(s1[r], s1[r + 1]);
      const u32 t0 = cvtpk(bflo(p0) * SCALE, bfhi(p0) * SCALE), t1 = cvtpk(bflo(p1) * SCALE, bfhi(p1) * SCALE);
      sc[r] = bflo(t0); sc[r + 1] = bfhi(t0); sc[4 + r] = bflo(t1); sc[4 + r + 1] = bfhi(t1);
    }
  }
  float mx = fmaxf(fmaxf(fmaxf(sc[0], sc[1]), fmaxf(sc[2], sc[3])), fmaxf(fmaxf(sc[4], sc[5]), fmaxf(sc[6], sc[7])));
  mx = x16_max(mx);
  LAS float* mxl = (LAS float*)(L + L_MX);
  if (g == 0 && (c & 3) == 0) mxl[(c >> 2) * 8 + w] = mx;
  BAR();
  float M;
  {
    const v4f m0 = *(LAS v4f*)(mxl + (c >> 2) * 8), m1 = *(LAS v4f*)(mxl + (c >> 2) * 8 + 4);
    M = fmaxf(fmaxf(fmaxf(m0.x, m0.y), fmaxf(m0.z, m0.w)), fmaxf(fmaxf(m1.x, m1.y), fmaxf(m1.z, m1.w)));
  }
  v4u P;
  {
    constexpr float L2E = 1.4426950408889634f;
    float e[8], ls = 0.f;
#pragma unroll
    for (int i = 0; i < 8; ++i) { e[i] = __builtin_amdgcn_exp2f((sc[i] - M) * L2E); ls += e[i]; }
    P = v4u{cvtpk(e[0], e[1]), cvtpk(e[2], e[3]), cvtpk(e[4], e[5]), cvtpk(e[6], e[7])};
    ls = x16_sum(ls);
    if (g == 0 && (c & 3) == 0) mxl[32 + (c >> 2) * 8 + w] = ls;
  }
  STAMP(6);
  {
    v4f o[8];
#pragma unroll
    for (int jj = 0; jj < 8; ++jj) {
      const u32 sel = (jj & 1) ? 0x07060302u : 0x05040100u;
      v4u bf;
#pragma unroll
      for (int pr = 0; pr < 4; ++pr) bf[pr] = __builtin_amdgcn_perm(V[2 * pr + 1][jj >> 1], V[2 * pr][jj >> 1], sel);
      o[jj] = mfma32(P, bf, v4f{0.f, 0.f, 0.f, 0.f});
    }
    LAS unsigned char* r2 = L + L_WT + w * 2048 + g * 512 + c * 32;
    *(LAS v4f*)r2 = v4f{o[0][0], o[1][0], o[2][0], o[3][0]};
    *(LAS v4f*)(r2 + 16) = v4f{o[4][0], o[5][0], o[6][0], o[7][0]};
  }
  STAMP(7);
  BAR();
  if (tid < 128) {
    const int hd = tid >> 5, dq = tid & 31;
    LAS v4f* r2 = (LAS v4f*)(L + L_WT + tid * 16);
    v4f o = r2[0];
#pragma unroll
    for (int ww = 1; ww < 8; ++ww) o += r2[ww * 128];
    const v4f l0 = *(LAS v4f*)(mxl + 32 + hd * 8), l1 = *(LAS v4f*)(mxl + 32 + hd * 8 + 4);
    const float Ls = ((l0.x + l0.y) + (l0.z + l0.w)) + ((l1.x + l1.y) + (l1.z + l1.w));
    const float inv = __builtin_amdgcn_rcpf(Ls);
    const u32 h32 = 4 * hkv + hd;
    STSC128((v4u{cvtpk(o.x * inv, o.y * inv), tag, cvtpk(o.z * inv, o.w * inv), tag}), rws, WS_PO + ((h32 * 8 + b) * 4 + s) * 512 + dq * 16, 0);
  }
  if (w == 2 && lane < 4) {
    const v4f l0 = *(LAS v4f*)(mxl + 32 + lane * 8), l1 = *(LAS v4f*)(mxl + 32 + lane * 8 + 4);
    const float Ls = ((l0.x + l0.y) + (l0.z + l0.w)) + ((l1.x + l1.y) + (l1.z + l1.w));
    const v4f m0 = *(LAS v4f*)(mxl + lane * 8), m1 = *(LAS v4f*)(mxl + lane * 8 + 4);
    const float Mh = fmaxf(fmaxf(fmaxf(m0.x, m0.y), fmaxf(m0.z, m0.w)), fmaxf(fmaxf(m1.x, m1.y), fmaxf(m1.z, m1.w)));
    STSC128((v4u{__float_as_uint(Mh), tag, __float_as_uint(Ls), tag}), rws, WS_ST + ((hkv * 8 + b) * 4 + s) * 128 + lane * 16, 0);
  }
  STAMP(8);
  {
    LAS unsigned char* wl = L + L_K + w * 8192;
    *(LAS v4u*)(wl + (lane >> 3) * 160 + (lane & 7) * 16) = wor[0]; *(LAS v4u*)(wl + (8 + (lane >> 3)) * 160 + (lane & 7) * 16) = wor[1];
    *(LAS v4u*)(wl + 2560 + (lane >> 3) * 160 + (lane & 7) * 16) = w2r[0]; *(LAS v4u*)(wl + 2560 + (8 + (lane >> 3)) * 160 + (lane & 7) * 16) = w2r[1];
    asm volatile("s_waitcnt lgkmcnt(0)" ::: "memory");
    v4u wa[4];
#pragma unroll
    for (int ct = 0; ct < 4; ++ct) {
      LAS unsigned char* tp = wl + (4 * g + (c >> 2)) * 160 + ct * 32 + (c & 3) * 8;
      const v2u t0 = trread(tp), t1 = trread(tp + 2560);
      wa[ct] = v4u{t0.x, t0.y, t1.x, t1.y};
    }
    const u32 poff = WS_PO + ((hh * 8 + w) * 4) * 512 + lane * 16;
    const u32 soff = WS_ST + ((hkv * 8 + w) * 4 + (lane >> 5)) * 128 + (hh & 3) * 16;
    const u32 hoff = WS_HG + ((ffn2 ? hh : 0) * 8 + w) * 512 + g * 128 + (c & 7) * 16;
    v4u po[2], st[2], hg = {0u, tag, 0u, tag};
    for (u32 spin = 0; spin < SPIN_CAP; ++spin) {
      asm volatile("" ::: "memory");
#pragma unroll
      for (int i = 0; i < 2; ++i) { po[i] = LDSC128(rws, poff + i * 1024, 0); st[i] = LDSC128(rws, soff + i * 256, 0); }
      if (ffn2) hg = LDSC128(rws, hoff, 0);
      bool ok = (hg.y == tag) & (hg.w == tag);
#pragma unroll
      for (int i = 0; i < 2; ++i) ok &= (po[i].y == tag) & (po[i].w == tag) & (st[i].y == tag) & (st[i].w == tag);
      if (__all(ok)) break;
    }
    STAMP(9);
    float Mg = fmaxf(__uint_as_float(st[0].x), __uint_as_float(st[1].x));
    { auto t = __builtin_amdgcn_permlane32_swap(__float_as_uint(Mg), __float_as_uint(Mg), false, false); Mg = fmaxf(__uint_as_float(t[0]), __uint_as_float(t[1])); }
    float ws_[2];
#pragma unroll
    for (int i = 0; i < 2; ++i) ws_[i] = __builtin_amdgcn_exp2f((__uint_as_float(st[i].x) - Mg) * 1.4426950408889634f) * __uint_as_float(st[i].z);
    float W = ws_[0] + ws_[1];
    { auto t = __builtin_amdgcn_permlane32_swap(__float_as_uint(W), __float_as_uint(W), false, false); W = __uint_as_float(t[0]) + __uint_as_float(t[1]); }
    const float iw = __builtin_amdgcn_rcpf(W);
    const float f0 = ws_[0] * iw, f1 = ws_[1] * iw;
    float ov[4] = {fmaf(f0, bflo(po[0].x), f1 * bflo(po[1].x)), fmaf(f0, bfhi(po[0].x), f1 * bfhi(po[1].x)),
                   fmaf(f0, bflo(po[0].z), f1 * bflo(po[1].z)), fmaf(f0, bfhi(po[0].z), f1 * bfhi(po[1].z))};
#pragma unroll
    for (int i = 0; i < 4; ++i) { auto t = __builtin_amdgcn_permlane32_swap(__float_as_uint(ov[i]), __float_as_uint(ov[i]), false, false); ov[i] = __uint_as_float(t[0]) + __uint_as_float(t[1]); }
    if (lane < 32) *(LAS v2u*)(L + L_OA + w * OA_STR + lane * 8) = v2u{cvtpk(ov[0], ov[1]), cvtpk(ov[2], ov[3])};
    BAR();
    const v2u oa = *(LAS v2u*)(L + L_OA + (c & 7) * OA_STR + (16 * w + 4 * g) * 2);
    const v4u Bf = {oa.x, oa.y, hg.x, hg.z};
    LAS unsigned char* r3 = L + L_WT + 16384 + w * 2176;
#pragma unroll
    for (int ct = 0; ct < 4; ++ct) {
      const v4f d = mfma32(wa[ct], Bf, v4f{0.f, 0.f, 0.f, 0.f});
      if (c < 8) *(LAS v4f*)(r3 + c * 272 + ct * 64 + g * 16) = d;
    }
  }
  BAR();
  if (tid < 256) {
    LAS v2f* r3 = (LAS v2f*)(L + L_WT + 16384 + (tid >> 5) * 272 + (tid & 31) * 8);
    v2f o = r3[0];
#pragma unroll
    for (int ww = 1; ww < 8; ++ww) o += r3[ww * 272];
    const int pb = tid >> 5, col = cs * 64 + (tid & 31) * 2;
    const u32 off = WS_P2 + (((pb * 32 + (col >> 4)) * 32 + hh) * 16 + (col & 15)) * 8;
    STSC128((v4u{__float_as_uint(o.x), tag, __float_as_uint(o.y), tag}), rws, off, 0);
  }
  STAMP(10);
  if (w == 0) {
    const int fb = bid & 7, c16 = bid >> 3;
    const u32 xo = fb * 1024 + c16 * 32 + (lane & 7) * 4;
    const u32 xp = __builtin_amdgcn_raw_buffer_load_b32(rx, xo, 0, 0);
    const u32 off = WS_P2 + ((fb * 32 + c16) * 32 + (lane >> 3)) * 128 + (lane & 7) * 16;
    v4u p[4];
    for (u32 spin = 0; spin < SPIN_CAP; ++spin) {
      asm volatile("" ::: "memory");
#pragma unroll
      for (int i = 0; i < 4; ++i) p[i] = LDSC128(rws, off + i * 1024, 0);
      bool ok = true;
#pragma unroll
      for (int i = 0; i < 4; ++i) ok &= (p[i].y == tag) & (p[i].w == tag);
      if (__all(ok)) break;
    }
    float s0 = (__uint_as_float(p[0].x) + __uint_as_float(p[1].x)) + (__uint_as_float(p[2].x) + __uint_as_float(p[3].x));
    float s1 = (__uint_as_float(p[0].z) + __uint_as_float(p[1].z)) + (__uint_as_float(p[2].z) + __uint_as_float(p[3].z));
    s0 += dpp<ROR8>(s0); s1 += dpp<ROR8>(s1);
    s0 = x16_sum(s0); s1 = x16_sum(s1);
    if (lane < 8) ((GAS u32*)a.out)[(fb * 512 + c16 * 16) / 2 + lane] = cvtpk(bflo(xp) + s0, bfhi(xp) + s1);
    if (bid == 0 && lane == 0) __hip_atomic_store((GAS u32*)(a.ws + WS_EPOCH), tag, __ATOMIC_RELAXED, __HIP_MEMORY_SCOPE_AGENT);
  }
  STAMP(11);
}
}

namespace fused {
__global__ void __launch_bounds__(576) dummy_k(unsigned* p) {
  __shared__ unsigned sm[31744];
  if (p == (unsigned*)1) { sm[threadIdx.x] = 1; __syncthreads(); p[0] = sm[(threadIdx.x + 1) % 31744]; }
}
}
extern "C" void kernel_launch(void* const* d_in, const int* in_sizes, int n_in, void* d_out, int out_size, void* d_ws, size_t ws_size, hipStream_t stream) {
  using namespace fused;
  Args a;
  a.x = (const u16*)d_in[0]; a.kc = (const u16*)d_in[1]; a.vc = (const u16*)d_in[2]; a.wq = (const u16*)d_in[3]; a.wo = (const u16*)d_in[4];
  a.w1 = (const u16*)d_in[6]; a.w2 = (const u16*)d_in[7]; a.out = (u16*)d_out; a.ws = (unsigned char*)d_ws;
  hipLaunchKernelGGL(fused_decode, dim3(256), dim3(NTHR), 0, stream, a);
  hipLaunchKernelGGL(dummy_k, dim3(256), dim3(576), 0, stream, (unsigned*)d_ws);
}
```

```cpp
#define PROBE 99
#define PROBE_BLK 0
#include <hip/hip_runtime.h>
#include <hip/hip_bf16.h>

namespace fused {
typedef unsigned u32; typedef unsigned long long u64; typedef unsigned short u16;
typedef u32 v4u __attribute__((ext_vector_type(4)));
typedef u32 v2u __attribute__((ext_vector_type(2)));
typedef float v4f __attribute__((ext_vector_type(4)));
typedef float v2f __attribute__((ext_vector_type(2)));
typedef __bf16 bf8 __attribute__((ext_vector_type(8)));
typedef short s4 __attribute__((ext_vector_type(4)));
#define LAS __attribute__((address_space(3)))
#define GAS __attribute__((address_space(1)))

constexpr int DM = 512, SK = 4096, DH = 128, WINK = 1024, FF = 1792, NQ = 4096;
constexpr int NWAVE = 9, NTHR = NWAVE * 64;
constexpr int AUX_NT = 2, AUX_SC1 = 16;
constexpr u32 RSRC3 = 0x00020000u;
constexpr u32 SPIN_CAP = 1u << 16;

constexpr u32 WS_EPOCH = 0;
constexpr u32 WS_QG = 0x1000;
constexpr u32 WS_HG = 0x41000;
constexpr u32 WS_ST = 0x50000;
constexpr u32 WS_PO = 0x60000;
constexpr u32 WS_P2 = 0xE0000;
constexpr u32 WS_END = 0x1E0000;
constexpr u32 WS_STAMP = 0x200000;

constexpr int L_XN = 0, XN_STR = 1040;
constexpr int L_WT = 8448;
constexpr int L_RED1 = L_WT + 32768;
constexpr int L_QL = L_RED1 + 16384, QL_STR = 320;
constexpr int L_MX = L_QL + 1280;
constexpr int L_TAG = L_MX + 256;
constexpr int L_OA = L_TAG + 16, OA_STR = 272;
constexpr int L_K = 61440;
constexpr int L_BYTES = L_K + 65536;
static_assert(L_OA + 8 * OA_STR + 96 <= L_K, "LDS map");

typedef __bf16 bf2 __attribute__((ext_vector_type(2)));
__device__ __forceinline__ u32 cvtpk(float lo, float hi) { return __builtin_bit_cast(u32, __builtin_convertvector((v2f{lo, hi}), bf2)); }
__device__ __forceinline__ float bflo(u32 p) { return __builtin_bit_cast(float, p << 16); }
__device__ __forceinline__ float bfhi(u32 p) { return __builtin_bit_cast(float, p & 0xffff0000u); }
__device__ __forceinline__ float rbf(float x) { return bflo(cvtpk(x, x)); }
template <int CTRL> __device__ __forceinline__ float dpp(float x) {
  return __builtin_bit_cast(float, __builtin_amdgcn_mov_dpp(__builtin_bit_cast(int, x), CTRL, 0xf, 0xf, true));
}
constexpr int XOR1 = 0xB1, XOR2 = 0x4E, HMIR = 0x141, RMIR = 0x140, ROR8 = 0x128;
__device__ __forceinline__ float x16_sum(float x) {
  auto s = __builtin_amdgcn_permlane16_swap(__float_as_uint(x), __float_as_uint(x), false, false);
  x = __uint_as_float(s[0]) + __uint_as_float(s[1]);
  auto t = __builtin_amdgcn_permlane32_swap(__float_as_uint(x), __float_as_uint(x), false, false);
  return __uint_as_float(t[0]) + __uint_as_float(t[1]);
}
__device__ __forceinline__ float x16_max(float x) {
  auto s = __builtin_amdgcn_permlane16_swap(__float_as_uint(x), __float_as_uint(x), false, false);
  x = fmaxf(__uint_as_float(s[0]), __uint_as_float(s[1]));
  auto t = __builtin_amdgcn_permlane32_swap(__float_as_uint(x), __float_as_uint(x), false, false);
  return fmaxf(__uint_as_float(t[0]), __uint_as_float(t[1]));
}
__device__ __forceinline__ float wave_sum(float x) {
  x += dpp<XOR1>(x); x += dpp<XOR2>(x); x += dpp<HMIR>(x); x += dpp<RMIR>(x); return x16_sum(x);
}
__device__ __forceinline__ v4f mfma32(v4u a, v4u b, v4f c) {
  return __builtin_amdgcn_mfma_f32_16x16x32_bf16(__builtin_bit_cast(bf8, a), __builtin_bit_cast(bf8, b), c, 0, 0, 0);
}
__device__ __forceinline__ v2u trread(LAS unsigned char* p) {
  typedef short v4s __attribute__((ext_vector_type(4)));
  return __builtin_bit_cast(v2u, __builtin_amdgcn_ds_read_tr16_b64_v4i16((LAS v4s*)p));
}
#define BAR() do { asm volatile("s_waitcnt lgkmcnt(0)" ::: "memory"); __builtin_amdgcn_s_barrier(); asm volatile("" ::: "memory"); } while (0)
#define SB() __builtin_amdgcn_sched_barrier(0)
__device__ __forceinline__ __amdgpu_buffer_rsrc_t mkrs(const void* p, u32 bytes) { return __builtin_amdgcn_make_buffer_rsrc((void*)p, 0, (int)bytes, RSRC3); }
__device__ __forceinline__ v4u ld128(__amdgpu_buffer_rsrc_t r, u32 vo, u32 so, int aux_is_nt) { return __builtin_amdgcn_raw_buffer_load_b128(r, vo, so, AUX_NT); }
#define LDNT128(r, vo, so) __builtin_amdgcn_raw_buffer_load_b128((r), (vo), (so), AUX_NT)
#define LDNT64(r, vo, so) __builtin_amdgcn_raw_buffer_load_b64((r), (vo), (so), AUX_NT)
#define LDSC128(r, vo, so) __builtin_amdgcn_raw_buffer_load_b128((r), (vo), (so), AUX_SC1)
#define STSC128(v, r, vo, so) __builtin_amdgcn_raw_buffer_store_b128((v), (r), (vo), (so), AUX_SC1)

#define STAMP(i) do { if ((i) == 0 || (i) == PROBE) { u64 t_; asm volatile("s_memrealtime %0\n\ts_waitcnt lgkmcnt(0)" : "=s"(t_) :: "memory"); if ((i) == 0) pt0 = t_; else pt1 = t_; } } while (0)

struct Args { const u16* x; const u16* kc; const u16* vc; const u16* wq; const u16* wo; const u16* w1; const u16* w2; u16* out; unsigned char* ws; };

__global__ void __launch_bounds__(NTHR, 3) fused_decode(Args a) {
  __shared__ __attribute__((aligned(16))) unsigned char smem[L_BYTES];
  LAS unsigned char* L = (LAS unsigned char*)smem;
  u64 pt0 = 0, pt1 = 0;
  STAMP(0);
  const int tid = threadIdx.x, lane = tid & 63, w = __builtin_amdgcn_readfirstlane(tid >> 6);
  const int bid = blockIdx.x, hkv = bid & 7, j = bid >> 3;
  const int g = lane >> 4, c = lane & 15;
  const int hh = 4 * hkv + (j & 3), cs = j >> 2;
  const bool ffn2 = hh < 14, ffn1 = (hh >= 14) && (hh < 28);
  asm volatile("" :: "s"(a.x), "s"(a.kc), "s"(a.vc), "s"(a.wq), "s"(a.wo), "s"(a.w1), "s"(a.w2), "s"(a.out), "s"(a.ws));
  const __amdgpu_buffer_rsrc_t rws = mkrs(a.ws, WS_END);
  const __amdgpu_buffer_rsrc_t rx = mkrs(a.x, 8192);
  const v4u xr = __builtin_amdgcn_raw_buffer_load_b128(mkrs(w == 8 ? (const void*)a.ws : (const void*)a.x, 8192), w == 8 ? WS_EPOCH : w * 1024 + lane * 16, 0, AUX_SC1);
  STAMP(1);
  SB(); __builtin_amdgcn_s_barrier(); SB();
  v4u wqr[2], w1r[2];
  {
    const __amdgpu_buffer_rsrc_t rwq = mkrs(a.wq, w < 8 ? DM * NQ * 2 : 0);
    const __amdgpu_buffer_rsrc_t rw1 = mkrs(a.w1, (ffn1 && w < 8) ? DM * FF * 2 : 0);
    const u32 vo = (64 * w + (lane >> 1)) * (NQ * 2) + (hkv * 512 + j * 16) * 2 + (lane & 1) * 16;
    wqr[0] = LDNT128(rwq, vo, 0); wqr[1] = LDNT128(rwq, vo, 32 * NQ * 2);
    const int t1 = ffn1 ? (hh - 14) * 8 + cs : 0;
    const u32 vo1 = (64 * w + (lane >> 1)) * (FF * 2) + t1 * 32 + (lane & 1) * 16;
    w1r[0] = LDNT128(rw1, vo1, 0); w1r[1] = LDNT128(rw1, vo1, 32 * FF * 2);
  }
  SB(); __builtin_amdgcn_s_barrier(); SB();

  if (w == 8) {
    const u32 tag = __builtin_amdgcn_readfirstlane(xr.x) + 1u;
    if (lane == 0) *(LAS u32*)(L + L_TAG) = tag;
    BAR();
    BAR();
    STAMP(2);
    {
      const int isH = lane >> 5;
      const int pb = isH ? (lane & 7) : (lane >> 2), pg = isH ? ((lane >> 3) & 3) : (lane & 3);
      LAS v4f* red = (LAS v4f*)(L + L_RED1 + isH * 8192) + (pg * 16 + pb);
      v4f s = red[0];
#pragma unroll
      for (int ww = 1; ww < 8; ++ww) s += red[ww * 64];
      v4u o;
      if (!isH) {
        o.x = cvtpk(s.x, s.y); o.z = cvtpk(s.z, s.w); o.y = tag; o.w = tag;
        STSC128(o, rws, WS_QG + ((pb * 8 + hkv) * 32 + j) * 128 + pg * 16, 0);
      } else if (ffn1) {
        float hv[4];
#pragma unroll
        for (int r = 0; r < 4; ++r) { const float hb = rbf(s[r]); hv[r] = hb * __builtin_amdgcn_rcpf(1.0f + __builtin_amdgcn_exp2f(-1.4426950408889634f * hb)); }
        o.x = cvtpk(hv[0], hv[1]); o.z = cvtpk(hv[2], hv[3]); o.y = tag; o.w = tag;
        const int t1 = (hh - 14) * 8 + cs;
        STSC128(o, rws, WS_HG + t1 * 512 + (lane - 32) * 16, 0);
      }
    }
    STAMP(3);
    {
      const int b = j >> 2;
      const u32 off = WS_QG + ((b * 8 + hkv) * 32 + (lane >> 2)) * 128 + (lane & 3) * 16;
      v4u q0, q1;
      for (u32 spin = 0; spin < SPIN_CAP; ++spin) {
        asm volatile("" ::: "memory");
        q0 = LDSC128(rws, off, 0); q1 = LDSC128(rws, off + 16 * 128, 0);
        const bool ok = (q0.y == tag) & (q0.w == tag) & (q1.y == tag) & (q1.w == tag);
        if (__all(ok)) break;
      }
      LAS unsigned char* ql = L + L_QL + (lane >> 5) * QL_STR + ((lane >> 2) & 7) * 32 + (lane & 3) * 8;
      *(LAS v2u*)ql = v2u{q0.x, q0.z};
      *(LAS v2u*)(ql + 2 * QL_STR) = v2u{q1.x, q1.z};
    }
    STAMP(4);
    BAR();
    return;
  }

  const int b = j >> 2, s = j & 3;
  const size_t kvbase = ((size_t)(b * 8 + hkv) * SK + (SK - WINK) + s * 256 + w * 32) * DH * 2;
  const __amdgpu_buffer_rsrc_t rk = mkrs((const unsigned char*)a.kc + kvbase, 8192);
  const __amdgpu_buffer_rsrc_t rv = mkrs((const unsigned char*)a.vc + kvbase, 8192);
  const __amdgpu_buffer_rsrc_t rwo = mkrs(a.wo, NQ * DM * 2);
  const __amdgpu_buffer_rsrc_t rw2 = mkrs(a.w2, ffn2 ? FF * DM * 2 : 0);
  v4u K[4], V[8];
  const u32 vvo = g * 1024 + c * 16;
  const u32 kvo = g * 256 + c * 16;
  LAS unsigned char* kl = L + L_K + w * 8192;
#define KLD(u) K[(u) & 3] = LDNT128(rk, kvo + ((u) & 3) * 1024, ((u) >> 2) * 4096)
#define KST(u) *(LAS v4u*)(kl + (4 * (u) + g) * 256 + ((c ^ ((4 * (u) + g) & 15)) * 16)) = K[(u) & 3]
#define VLD(i) V[i] = LDNT128(rv, vvo + ((i) & 3) * 256, ((i) >> 2) * 4096)
  KLD(0); KLD(1); KLD(2); KLD(3);
  SB();
  {
    float xv[8];
#pragma unroll
    for (int i = 0; i < 4; ++i) { xv[2 * i] = bflo(xr[i]); xv[2 * i + 1] = bfhi(xr[i]); }
    float sm = ((xv[0] + xv[1]) + (xv[2] + xv[3])) + ((xv[4] + xv[5]) + (xv[6] + xv[7]));
    sm = wave_sum(sm);
    const float mean = sm * (1.0f / DM);
    float ss = 0.f;
#pragma unroll
    for (int i = 0; i < 8; ++i) { const float d = xv[i] - mean; ss = fmaf(d, d, ss); }
    ss = wave_sum(ss);
    const float mb = rbf(mean), var = rbf(ss * (1.0f / DM));
    const float den = rbf(__builtin_amdgcn_sqrtf(rbf(var + 1e-5f)));
    const float inv = __builtin_amdgcn_rcpf(den);
    v4u o;
#pragma unroll
    for (int i = 0; i < 4; ++i) {
      const u32 dd = cvtpk(xv[2 * i] - mb, xv[2 * i + 1] - mb);
      o[i] = cvtpk(bflo(dd) * inv, bfhi(dd) * inv);
    }
    *(LAS v4u*)(L + L_XN + w * XN_STR + lane * 16) = o;
  }
  STAMP(2);
  BAR();
  {
    LAS unsigned char* wt = L + L_WT + w * 2048;
    *(LAS v4u*)(wt + lane * 16) = wqr[0]; *(LAS v4u*)(wt + 1024 + lane * 16) = wqr[1];
    *(LAS v4u*)(wt + 16384 + lane * 16) = w1r[0]; *(LAS v4u*)(wt + 16384 + 1024 + lane * 16) = w1r[1];
    asm volatile("s_waitcnt lgkmcnt(0)" ::: "memory");
    v4u xb[2];
#pragma unroll
    for (int t = 0; t < 2; ++t) {
      LAS unsigned char* xp = L + L_XN + (c & 7) * XN_STR + (64 * w + 32 * t + 4 * g) * 2;
      const v2u lo = *(LAS v2u*)xp, hi = *(LAS v2u*)(xp + 32);
      xb[t] = v4u{lo.x, lo.y, hi.x, hi.y};
    }
    v4f aq = {0.f, 0.f, 0.f, 0.f}, ah = {0.f, 0.f, 0.f, 0.f};
#pragma unroll
    for (int t = 0; t < 2; ++t) {
      const v2u q0 = trread(wt + t * 1024 + lane * 8), q1 = trread(wt + t * 1024 + 512 + lane * 8);
      aq = mfma32(v4u{q0.x, q0.y, q1.x, q1.y}, xb[t], aq);
      const v2u h0 = trread(wt + 16384 + t * 1024 + lane * 8), h1 = trread(wt + 16384 + t * 1024 + 512 + lane * 8);
      ah = mfma32(v4u{h0.x, h0.y, h1.x, h1.y}, xb[t], ah);
    }
    *(LAS v4f*)(L + L_RED1 + w * 1024 + lane * 16) = aq;
    *(LAS v4f*)(L + L_RED1 + 8192 + w * 1024 + lane * 16) = ah;
  }
  STAMP(3);
  BAR();
#define PIN(x) asm volatile("" : "+v"(x))
  KST(0); SB(); KLD(4); SB(); KST(1); SB(); KLD(5); SB(); KST(2); SB(); KLD(6); SB(); KST(3); SB(); KLD(7); SB();
  KST(4); SB(); VLD(0); SB(); KST(5); SB(); VLD(1); SB(); KST(6); SB(); VLD(2); SB(); KST(7); SB(); VLD(3); SB();
  PIN(V[0]); VLD(4); SB(); PIN(V[1]); VLD(5); SB(); PIN(V[2]); VLD(6); SB(); PIN(V[3]); VLD(7); SB();
  v4u wor[2], w2r[2];
  {
    const u32 vo = (hh * 128 + 16 * w + (lane >> 3)) * (DM * 2) + cs * 128 + (lane & 7) * 16;
    const u32 vo2 = ((ffn2 ? hh : 0) * 128 + 16 * w + (lane >> 3)) * (DM * 2) + cs * 128 + (lane & 7) * 16;
    wor[0] = LDNT128(rwo, vo, 0); wor[1] = LDNT128(rwo, vo, 8 * DM * 2);
    w2r[0] = LDNT128(rw2, vo2, 0); w2r[1] = LDNT128(rw2, vo2, 8 * DM * 2);
  }
  SB();
  const u32 tag = *(LAS u32*)(L + L_TAG);
  STAMP(4);
  BAR();
  STAMP(5);
  float sc[8];
  {
    v4u qf[4];
#pragma unroll
    for (int i = 0; i < 4; ++i) qf[i] = *(LAS v4u*)(L + L_QL + (c >> 2) * QL_STR + i * 64 + g * 16);
    v4f s0 = {0.f, 0.f, 0.f, 0.f}, s1 = s0;
#pragma unroll
    for (int i = 0; i < 4; ++i) {
      const v4u k0 = *(LAS v4u*)(kl + c * 256 + (((4 * i + g) ^ c) * 16)), k1 = *(LAS v4u*)(kl + 4096 + c * 256 + (((4 * i + g) ^ c) * 16));
      s0 = mfma32(k0, qf[i], s0); s1 = mfma32(k1, qf[i], s1);
    }
    constexpr float SCALE = 0.08838834764831845f;
#pragma unroll
    for (int r = 0; r < 4; r += 2) {
      const u32 p0 = cvtpk(s0[r], s0[r + 1]), p1 = cvtpk(s1[r], s1[r + 1]);
      const u32 t0 = cvtpk(bflo(p0) * SCALE, bfhi(p0) * SCALE), t1 = cvtpk(bflo(p1) * SCALE, bfhi(p1) * SCALE);
      sc[r] = bflo(t0); sc[r + 1] = bfhi(t0); sc[4 + r] = bflo(t1); sc[4 + r + 1] = bfhi(t1);
    }
  }
  float mx = fmaxf(fmaxf(fmaxf(sc[0], sc[1]), fmaxf(sc[2], sc[3])), fmaxf(fmaxf(sc[4], sc[5]), fmaxf(sc[6], sc[7])));
  mx = x16_max(mx);
  LAS float* mxl = (LAS float*)(L + L_MX);
  if (g == 0 && (c & 3) == 0) mxl[(c >> 2) * 8 + w] = mx;
  BAR();
  float M;
  {
    const v4f m0 = *(LAS v4f*)(mxl + (c >> 2) * 8), m1 = *(LAS v4f*)(mxl + (c >> 2) * 8 + 4);
    M = fmaxf(fmaxf(fmaxf(m0.x, m0.y), fmaxf(m0.z, m0.w)), fmaxf(fmaxf(m1.x, m1.y), fmaxf(m1.z, m1.w)));
  }
  v4u P;
  {
    constexpr float L2E = 1.4426950408889634f;
    float e[8], ls = 0.f;
#pragma unroll
    for (int i = 0; i < 8; ++i) { e[i] = __builtin_amdgcn_exp2f((sc[i] - M) * L2E); ls += e[i]; }
    P = v4u{cvtpk(e[0], e[1]), cvtpk(e[2], e[3]), cvtpk(e[4], e[5]), cvtpk(e[6], e[7])};
    ls = x16_sum(ls);
    if (g == 0 && (c & 3) == 0) mxl[32 + (c >> 2) * 8 + w] = ls;
  }
  STAMP(6);
  {
    v4f o[8];
#pragma unroll
    for (int jj = 0; jj < 8; ++jj) {
      const u32 sel = (jj & 1) ? 0x07060302u : 0x05040100u;
      v4u bf;
#pragma unroll
      for (int pr = 0; pr < 4; ++pr) bf[pr] = __builtin_amdgcn_perm(V[2 * pr + 1][jj >> 1], V[2 * pr][jj >> 1], sel);
      o[jj] = mfma32(P, bf, v4f{0.f, 0.f, 0.f, 0.f});
    }
    LAS unsigned char* r2 = L + L_WT + w * 2048 + g * 512 + c * 32;
    *(LAS v4f*)r2 = v4f{o[0][0], o[1][0], o[2][0], o[3][0]};
    *(LAS v4f*)(r2 + 16) = v4f{o[4][0], o[5][0], o[6][0], o[7][0]};
  }
  STAMP(7);
  BAR();
  if (tid < 128) {
    const int hd = tid >> 5, dq = tid & 31;
    LAS v4f* r2 = (LAS v4f*)(L + L_WT + tid * 16);
    v4f o = r2[0];
#pragma unroll
    for (int ww = 1; ww < 8; ++ww) o += r2[ww * 128];
    const v4f l0 = *(LAS v4f*)(mxl + 32 + hd * 8), l1 = *(LAS v4f*)(mxl + 32 + hd * 8 + 4);
    const float Ls = ((l0.x + l0.y) + (l0.z + l0.w)) + ((l1.x + l1.y) + (l1.z + l1.w));
    const float inv = __builtin_amdgcn_rcpf(Ls);
    const u32 h32 = 4 * hkv + hd;
    STSC128((v4u{cvtpk(o.x * inv, o.y * inv), tag, cvtpk(o.z * inv, o.w * inv), tag}), rws, WS_PO + ((h32 * 8 + b) * 4 + s) * 512 + dq * 16, 0);
  }
  if (w == 2 && lane < 4) {
    const v4f l0 = *(LAS v4f*)(mxl + 32 + lane * 8), l1 = *(LAS v4f*)(mxl + 32 + lane * 8 + 4);
    const float Ls = ((l0.x + l0.y) + (l0.z + l0.w)) + ((l1.x + l1.y) + (l1.z + l1.w));
    const v4f m0 = *(LAS v4f*)(mxl + lane * 8), m1 = *(LAS v4f*)(mxl + lane * 8 + 4);
    const float Mh = fmaxf(fmaxf(fmaxf(m0.x, m0.y), fmaxf(m0.z, m0.w)), fmaxf(fmaxf(m1.x, m1.y), fmaxf(m1.z, m1.w)));
    STSC128((v4u{__float_as_uint(Mh), tag, __float_as_uint(Ls), tag}), rws, WS_ST + ((hkv * 8 + b) * 4 + s) * 128 + lane * 16, 0);
  }
  STAMP(8);
  {
    LAS unsigned char* wl = L + L_K + w * 8192;
    *(LAS v4u*)(wl + (lane >> 3) * 160 + (lane & 7) * 16) = wor[0]; *(LAS v4u*)(wl + (8 + (lane >> 3)) * 160 + (lane & 7) * 16) = wor[1];
    *(LAS v4u*)(wl + 2560 + (lane >> 3) * 160 + (lane & 7) * 16) = w2r[0]; *(LAS v4u*)(wl + 2560 + (8 + (lane >> 3)) * 160 + (lane & 7) * 16) = w2r[1];
    asm volatile("s_waitcnt lgkmcnt(0)" ::: "memory");
    v4u wa[4];
#pragma unroll
    for (int ct = 0; ct < 4; ++ct) {
      LAS unsigned char* tp = wl + (4 * g + (c >> 2)) * 160 + ct * 32 + (c & 3) * 8;
      const v2u t0 = trread(tp), t1 = trread(tp + 2560);
      wa[ct] = v4u{t0.x, t0.y, t1.x, t1.y};
    }
    const u32 poff = WS_PO + ((hh * 8 + w) * 4) * 512 + lane * 16;
    const u32 soff = WS_ST + ((hkv * 8 + w) * 4 + (lane >> 5)) * 128 + (hh & 3) * 16;
    const u32 hoff = WS_HG + ((ffn2 ? hh : 0) * 8 + w) * 512 + g * 128 + (c & 7) * 16;
    v4u po[2], st[2], hg = {0u, tag, 0u, tag};
    for (u32 spin = 0; spin < SPIN_CAP; ++spin) {
      asm volatile("" ::: "memory");
#pragma unroll
      for (int i = 0; i < 2; ++i) { po[i] = LDSC128(rws, poff + i * 1024, 0); st[i] = LDSC128(rws, soff + i * 256, 0); }
      if (ffn2) hg = LDSC128(rws, hoff, 0);
      bool ok = (hg.y == tag) & (hg.w == tag);
#pragma unroll
      for (int i = 0; i < 2; ++i) ok &= (po[i].y == tag) & (po[i].w == tag) & (st[i].y == tag) & (st[i].w == tag);
      if (__all(ok)) break;
    }
    STAMP(9);
    float Mg = fmaxf(__uint_as_float(st[0].x), __uint_as_float(st[1].x));
    { auto t = __builtin_amdgcn_permlane32_swap(__float_as_uint(Mg), __float_as_uint(Mg), false, false); Mg = fmaxf(__uint_as_float(t[0]), __uint_as_float(t[1])); }
    float ws_[2];
#pragma unroll
    for (int i = 0; i < 2; ++i) ws_[i] = __builtin_amdgcn_exp2f((__uint_as_float(st[i].x) - Mg) * 1.4426950408889634f) * __uint_as_float(st[i].z);
    float W = ws_[0] + ws_[1];
    { auto t = __builtin_amdgcn_permlane32_swap(__float_as_uint(W), __float_as_uint(W), false, false); W = __uint_as_float(t[0]) + __uint_as_float(t[1]); }
    const float iw = __builtin_amdgcn_rcpf(W);
    const float f0 = ws_[0] * iw, f1 = ws_[1] * iw;
    float ov[4] = {fmaf(f0, bflo(po[0].x), f1 * bflo(po[1].x)), fmaf(f0, bfhi(po[0].x), f1 * bfhi(po[1].x)),
                   fmaf(f0, bflo(po[0].z), f1 * bflo(po[1].z)), fmaf(f0, bfhi(po[0].z), f1 * bfhi(po[1].z))};
#pragma unroll
    for (int i = 0; i < 4; ++i) { auto t = __builtin_amdgcn_permlane32_swap(__float_as_uint(ov[i]), __float_as_uint(ov[i]), false, false); ov[i] = __uint_as_float(t[0]) + __uint_as_float(t[1]); }
    if (lane < 32) *(LAS v2u*)(L + L_OA + w * OA_STR + lane * 8) = v2u{cvtpk(ov[0], ov[1]), cvtpk(ov[2], ov[3])};
    BAR();
    const v2u oa = *(LAS v2u*)(L + L_OA + (c & 7) * OA_STR + (16 * w + 4 * g) * 2);
    const v4u Bf = {oa.x, oa.y, hg.x, hg.z};
    LAS unsigned char* r3 = L + L_WT + 16384 + w * 2176;
#pragma unroll
    for (int ct = 0; ct < 4; ++ct) {
      const v4f d = mfma32(wa[ct], Bf, v4f{0.f, 0.f, 0.f, 0.f});
      if (c < 8) *(LAS v4f*)(r3 + c * 272 + ct * 64 + g * 16) = d;
    }
  }
  BAR();
  if (tid < 256) {
    LAS v2f* r3 = (LAS v2f*)(L + L_WT + 16384 + (tid >> 5) * 272 + (tid & 31) * 8);
    v2f o = r3[0];
#pragma unroll
    for (int ww = 1; ww < 8; ++ww) o += r3[ww * 272];
    const int pb = tid >> 5, col = cs * 64 + (tid & 31) * 2;
    const u32 off = WS_P2 + (((pb * 32 + (col >> 4)) * 32 + hh) * 16 + (col & 15)) * 8;
    STSC128((v4u{__float_as_uint(o.x), tag, __float_as_uint(o.y), tag}), rws, off, 0);
  }
  STAMP(10);
  if (w == 0) {
    const int fb = bid & 7, c16 = bid >> 3;
    const u32 xo = fb * 1024 + c16 * 32 + (lane & 7) * 4;
    const u32 xp = __builtin_amdgcn_raw_buffer_load_b32(rx, xo, 0, 0);
    const u32 off = WS_P2 + ((fb * 32 + c16) * 32 + (lane >> 3)) * 128 + (lane & 7) * 16;
    v4u p[4];
    for (u32 spin = 0; spin < SPIN_CAP; ++spin) {
      asm volatile("" ::: "memory");
#pragma unroll
      for (int i = 0; i < 4; ++i) p[i] = LDSC128(rws, off + i * 1024, 0);
      bool ok = true;
#pragma unroll
      for (int i = 0; i < 4; ++i) ok &= (p[i].y == tag) & (p[i].w == tag);
      if (__all(ok)) break;
    }
    float s0 = (__uint_as_float(p[0].x) + __uint_as_float(p[1].x)) + (__uint_as_float(p[2].x) + __uint_as_float(p[3].x));
    float s1 = (__uint_as_float(p[0].z) + __uint_as_float(p[1].z)) + (__uint_as_float(p[2].z) + __uint_as_float(p[3].z));
    s0 += dpp<ROR8>(s0); s1 += dpp<ROR8>(s1);
    s0 = x16_sum(s0); s1 = x16_sum(s1);
    if (lane < 8) ((GAS u32*)a.out)[(fb * 512 + c16 * 16) / 2 + lane] = cvtpk(bflo(xp) + s0, bfhi(xp) + s1);
    if (bid == 0 && lane == 0) __hip_atomic_store((GAS u32*)(a.ws + WS_EPOCH), tag, __ATOMIC_RELAXED, __HIP_MEMORY_SCOPE_AGENT);
  }
  STAMP(11);
  if (bid == PROBE_BLK && w == 0) {
    const u64 td = __hip_atomic_load((GAS u64*)(a.ws + 0x300000), __ATOMIC_RELAXED, __HIP_MEMORY_SCOPE_AGENT);
    const u64 tdu = ((u64)__builtin_amdgcn_readfirstlane((u32)(td >> 32)) << 32) | (u32)__builtin_amdgcn_readfirstlane((u32)td);
    u64 d = pt0 - tdu; if (d > 5000) d = 5000;
    u64 t_; asm volatile("s_memrealtime %0\n\ts_waitcnt lgkmcnt(0)" : "=s"(t_) :: "memory");
    const u64 until = t_ + 0 * d;
    for (int it = 0; it < 100000; ++it) { u64 n_; asm volatile("s_memrealtime %0\n\ts_waitcnt lgkmcnt(0)" : "=s"(n_) :: "memory"); if (n_ >= until) break; }
  }
}
}

namespace fused {
__global__ void __launch_bounds__(64) dummy_k(unsigned long long* p) {
  u64 t_; asm volatile("s_memrealtime %0\n\ts_waitcnt lgkmcnt(0)" : "=s"(t_) :: "memory");
  if (threadIdx.x == 0) p[0] = t_;
}
}
extern "C" void kernel_launch(void* const* d_in, const int* in_sizes, int n_in, void* d_out, int out_size, void* d_ws, size_t ws_size, hipStream_t stream) {
  using namespace fused;
  Args a;
  a.x = (const u16*)d_in[0]; a.kc = (const u16*)d_in[1]; a.vc = (const u16*)d_in[2]; a.wq = (const u16*)d_in[3]; a.wo = (const u16*)d_in[4];
  a.w1 = (const u16*)d_in[6]; a.w2 = (const u16*)d_in[7]; a.out = (u16*)d_out; a.ws = (unsigned char*)d_ws;
  hipLaunchKernelGGL(dummy_k, dim3(1), dim3(64), 0, stream, (unsigned long long*)((char*)d_ws + 0x300000));
  hipLaunchKernelGGL(fused_decode, dim3(256), dim3(NTHR), 0, stream, a);
}
```

```cpp
#include <hip/hip_runtime.h>
#include <hip/hip_bf16.h>

namespace fused {
typedef unsigned u32; typedef unsigned long long u64; typedef unsigned short u16;
typedef u32 v4u __attribute__((ext_vector_type(4)));
typedef u32 v2u __attribute__((ext_vector_type(2)));
typedef float v4f __attribute__((ext_vector_type(4)));
typedef float v2f __attribute__((ext_vector_type(2)));
typedef __bf16 bf8 __attribute__((ext_vector_type(8)));
typedef short s4 __attribute__((ext_vector_type(4)));
#define LAS __attribute__((address_space(3)))
#define GAS __attribute__((address_space(1)))

constexpr int DM = 512, SK = 4096, DH = 128, WINK = 1024, FF = 1792, NQ = 4096;
constexpr int NWAVE = 9, NTHR = NWAVE * 64;
constexpr int AUX_NT = 2, AUX_SC1 = 16;
constexpr u32 RSRC3 = 0x00020000u;
constexpr u32 SPIN_CAP = 1u << 16;

constexpr u32 WS_EPOCH = 0;
constexpr u32 WS_QG = 0x1000;
constexpr u32 WS_HG = 0x41000;
constexpr u32 WS_ST = 0x50000;
constexpr u32 WS_PO = 0x60000;
constexpr u32 WS_P2 = 0xE0000;
constexpr u32 WS_END = 0x1E0000;
constexpr u32 WS_STAMP = 0x200000;

constexpr int L_XN = 0, XN_STR = 1040;
constexpr int L_WT = 8448;
constexpr int L_RED1 = L_WT + 32768;
constexpr int L_QL = L_RED1 + 16384, QL_STR = 320;
constexpr int L_MX = L_QL + 1280;
constexpr int L_TAG = L_MX + 256;
constexpr int L_OA = L_TAG + 16, OA_STR = 272;
constexpr int L_K = 61440;
constexpr int L_BYTES = L_K + 65536;
static_assert(L_OA + 8 * OA_STR + 96 <= L_K, "LDS map");

typedef __bf16 bf2 __attribute__((ext_vector_type(2)));
__device__ __forceinline__ u32 cvtpk(float lo, float hi) { return __builtin_bit_cast(u32, __builtin_convertvector((v2f{lo, hi}), bf2)); }
__device__ __forceinline__ float bflo(u32 p) { return __builtin_bit_cast(float, p << 16); }
__device__ __forceinline__ float bfhi(u32 p) { return __builtin_bit_cast(float, p & 0xffff0000u); }
__device__ __forceinline__ float rbf(float x) { return bflo(cvtpk(x, x)); }
template <int CTRL> __device__ __forceinline__ float dpp(float x) {
  return __builtin_bit_cast(float, __builtin_amdgcn_mov_dpp(__builtin_bit_cast(int, x), CTRL, 0xf, 0xf, true));
}
constexpr int XOR1 = 0xB1, XOR2 = 0x4E, HMIR = 0x141, RMIR = 0x140, ROR8 = 0x128;
__device__ __forceinline__ float x16_sum(float x) {
  auto s = __builtin_amdgcn_permlane16_swap(__float_as_uint(x), __float_as_uint(x), false, false);
  x = __uint_as_float(s[0]) + __uint_as_float(s[1]);
  auto t = __builtin_amdgcn_permlane32_swap(__float_as_uint(x), __float_as_uint(x), false, false);
  return __uint_as_float(t[0]) + __uint_as_float(t[1]);
}
__device__ __forceinline__ float x16_max(float x) {
  auto s = __builtin_amdgcn_permlane16_swap(__float_as_uint(x), __float_as_uint(x), false, false);
  x = fmaxf(__uint_as_float(s[0]), __uint_as_float(s[1]));
  auto t = __builtin_amdgcn_permlane32_swap(__float_as_uint(x), __float_as_uint(x), false, false);
  return fmaxf(__uint_as_float(t[0]), __uint_as_float(t[1]));
}
__device__ __forceinline__ float wave_sum(float x) {
  x += dpp<XOR1>(x); x += dpp<XOR2>(x); x += dpp<HMIR>(x); x += dpp<RMIR>(x); return x16_sum(x);
}
__device__ __forceinline__ v4f mfma32(v4u a, v4u b, v4f c) {
  return __builtin_amdgcn_mfma_f32_16x16x32_bf16(__builtin_bit_cast(bf8, a), __builtin_bit_cast(bf8, b), c, 0, 0, 0);
}
__device__ __forceinline__ v2u trread(LAS unsigned char* p) {
  typedef short v4s __attribute__((ext_vector_type(4)));
  return __builtin_bit_cast(v2u, __builtin_amdgcn_ds_read_tr16_b64_v4i16((LAS v4s*)p));
}
#define BAR() do { asm volatile("s_waitcnt lgkmcnt(0)" ::: "memory"); __builtin_amdgcn_s_barrier(); asm volatile("" ::: "memory"); } while (0)
#define SB() __builtin_amdgcn_sched_barrier(0)
__device__ __forceinline__ __amdgpu_buffer_rsrc_t mkrs(const void* p, u32 bytes) { return __builtin_amdgcn_make_buffer_rsrc((void*)p, 0, (int)bytes, RSRC3); }
__device__ __forceinline__ v4u ld128(__amdgpu_buffer_rsrc_t r, u32 vo, u32 so, int aux_is_nt) { return __builtin_amdgcn_raw_buffer_load_b128(r, vo, so, AUX_NT); }
#define LDNT128(r, vo, so) __builtin_amdgcn_raw_buffer_load_b128((r), (vo), (so), AUX_NT)
#define LDNT64(r, vo, so) __builtin_amdgcn_raw_buffer_load_b64((r), (vo), (so), AUX_NT)
#define LDSC128(r, vo, so) __builtin_amdgcn_raw_buffer_load_b128((r), (vo), (so), AUX_SC1)
#define STSC128(v, r, vo, so) __builtin_amdgcn_raw_buffer_store_b128((v), (r), (vo), (so), AUX_SC1)

#define STAMP(i) do { } while (0)

struct Args { const u16* x; const u16* kc; const u16* vc; const u16* wq; const u16* wo; const u16* w1; const u16* w2; u16* out; unsigned char* ws; };

__global__ void __launch_bounds__(NTHR, 3) fused_decode(Args a) {
  __shared__ __attribute__((aligned(16))) unsigned char smem[L_BYTES];
  LAS unsigned char* L = (LAS unsigned char*)smem;
  STAMP(0);
  const int tid = threadIdx.x, lane = tid & 63, w = __builtin_amdgcn_readfirstlane(tid >> 6);
  const int bid = blockIdx.x, hkv = bid & 7, j = bid >> 3;
  const int g = lane >> 4, c = lane & 15;
  const int hh = 4 * hkv + (j & 3), cs = j >> 2;
  const bool ffn2 = hh < 14, ffn1 = (hh >= 14) && (hh < 28);
  asm volatile("" :: "s"(a.x), "s"(a.kc), "s"(a.vc), "s"(a.wq), "s"(a.wo), "s"(a.w1), "s"(a.w2), "s"(a.out), "s"(a.ws));
  const __amdgpu_buffer_rsrc_t rws = mkrs(a.ws, WS_END);
  const __amdgpu_buffer_rsrc_t rx = mkrs(a.x, 8192);
  const v4u xr = __builtin_amdgcn_raw_buffer_load_b128(mkrs(w == 8 ? (const void*)a.ws : (const void*)a.x, 8192), w == 8 ? WS_EPOCH : w * 1024 + lane * 16, 0, AUX_SC1);
  STAMP(1);
  SB(); __builtin_amdgcn_s_barrier(); SB();
  v4u wqr[2], w1r[2];
  {
    const __amdgpu_buffer_rsrc_t rwq = mkrs(a.wq, w < 8 ? DM * NQ * 2 : 0);
    const __amdgpu_buffer_rsrc_t rw1 = mkrs(a.w1, (ffn1 && w < 8) ? DM * FF * 2 : 0);
    const u32 vo = (64 * w + (lane >> 1)) * (NQ * 2) + (hkv * 512 + j * 16) * 2 + (lane & 1) * 16;
    wqr[0] = LDNT128(rwq, vo, 0); wqr[1] = LDNT128(rwq, vo, 32 * NQ * 2);
    const int t1 = ffn1 ? (hh - 14) * 8 + cs : 0;
    const u32 vo1 = (64 * w + (lane >> 1)) * (FF * 2) + t1 * 32 + (lane & 1) * 16;
    w1r[0] = LDNT128(rw1, vo1, 0); w1r[1] = LDNT128(rw1, vo1, 32 * FF * 2);
  }
  SB(); __builtin_amdgcn_s_barrier(); SB();

  if (w == 8) {
    const u32 tag = __builtin_amdgcn_readfirstlane(xr.x) + 1u;
    if (lane == 0) *(LAS u32*)(L + L_TAG) = tag;
    BAR();
    BAR();
    STAMP(2);
    {
      const int isH = lane >> 5;
      const int pb = isH ? (lane & 7) : (lane >> 2), pg = isH ? ((lane >> 3) & 3) : (lane & 3);
      LAS v4f* red = (LAS v4f*)(L + L_RED1 + isH * 8192) + (pg * 16 + pb);
      v4f s = red[0];
#pragma unroll
      for (int ww = 1; ww < 8; ++ww) s += red[ww * 64];
      v4u o;
      if (!isH) {
        o.x = cvtpk(s.x, s.y); o.z = cvtpk(s.z, s.w); o.y = tag; o.w = tag;
        STSC128(o, rws, WS_QG + ((pb * 8 + hkv) * 32 + j) * 128 + pg * 16, 0);
      } else if (ffn1) {
        float hv[4];
#pragma unroll
        for (int r = 0; r < 4; ++r) { const float hb = rbf(s[r]); hv[r] = hb * __builtin_amdgcn_rcpf(1.0f + __builtin_amdgcn_exp2f(-1.4426950408889634f * hb)); }
        o.x = cvtpk(hv[0], hv[1]); o.z = cvtpk(hv[2], hv[3]); o.y = tag; o.w = tag;
        const int t1 = (hh - 14) * 8 + cs;
        STSC128(o, rws, WS_HG + t1 * 512 + (lane - 32) * 16, 0);
      }
    }
    STAMP(3);
    {
      const int b = j >> 2;
      const u32 off = WS_QG + ((b * 8 + hkv) * 32 + (lane >> 2)) * 128 + (lane & 3) * 16;
      v4u q0, q1;
      for (u32 spin = 0; spin < SPIN_CAP; ++spin) {
        asm volatile("" ::: "memory");
        q0 = LDSC128(rws, off, 0); q1 = LDSC128(rws, off + 16 * 128, 0);
        const bool ok = (q0.y == tag) & (q0.w == tag) & (q1.y == tag) & (q1.w == tag);
        if (__all(ok)) break;
      }
      LAS unsigned char* ql = L + L_QL + (lane >> 5) * QL_STR + ((lane >> 2) & 7) * 32 + (lane & 3) * 8;
      *(LAS v2u*)ql = v2u{q0.x, q0.z};
      *(LAS v2u*)(ql + 2 * QL_STR) = v2u{q1.x, q1.z};
    }
    STAMP(4);
    BAR();
    return;
  }

  const int b = j >> 2, s = j & 3;
  const size_t kvbase = ((size_t)(b * 8 + hkv) * SK + (SK - WINK) + s * 256 + w * 32) * DH * 2;
  const __amdgpu_buffer_rsrc_t rk = mkrs((const unsigned char*)a.kc + kvbase, 8192);
  const __amdgpu_buffer_rsrc_t rv = mkrs((const unsigned char*)a.vc + kvbase, 8192);
  const __amdgpu_buffer_rsrc_t rwo = mkrs(a.wo, NQ * DM * 2);
  const __amdgpu_buffer_rsrc_t rw2 = mkrs(a.w2, ffn2 ? FF * DM * 2 : 0);
  v4u K[4], V[8];
  const u32 vvo = g * 1024 + c * 16;
  const u32 kvo = g * 256 + c * 16;
  LAS unsigned char* kl = L + L_K + w * 8192;
#define KLD(u) K[(u) & 3] = LDNT128(rk, kvo + ((u) & 3) * 1024, ((u) >> 2) * 4096)
#define KST(u) *(LAS v4u*)(kl + (4 * (u) + g) * 256 + ((c ^ ((4 * (u) + g) & 15)) * 16)) = K[(u) & 3]
#define VLD(i) V[i] = LDNT128(rv, vvo + ((i) & 3) * 256, ((i) >> 2) * 4096)
  KLD(0); KLD(1); KLD(2); KLD(3);
  SB();
  {
    float xv[8];
#pragma unroll
    for (int i = 0; i < 4; ++i) { xv[2 * i] = bflo(xr[i]); xv[2 * i + 1] = bfhi(xr[i]); }
    float sm = ((xv[0] + xv[1]) + (xv[2] + xv[3])) + ((xv[4] + xv[5]) + (xv[6] + xv[7]));
    sm = wave_sum(sm);
    const float mean = sm * (1.0f / DM);
    float ss = 0.f;
#pragma unroll
    for (int i = 0; i < 8; ++i) { const float d = xv[i] - mean; ss = fmaf(d, d, ss); }
    ss = wave_sum(ss);
    const float mb = rbf(mean), var = rbf(ss * (1.0f / DM));
    const float den = rbf(__builtin_amdgcn_sqrtf(rbf(var + 1e-5f)));
    const float inv = __builtin_amdgcn_rcpf(den);
    v4u o;
#pragma unroll
    for (int i = 0; i < 4; ++i) {
      const u32 dd = cvtpk(xv[2 * i] - mb, xv[2 * i + 1] - mb);
      o[i] = cvtpk(bflo(dd) * inv, bfhi(dd) * inv);
    }
    *(LAS v4u*)(L + L_XN + w * XN_STR + lane * 16) = o;
  }
  STAMP(2);
  BAR();
  {
    LAS unsigned char* wt = L + L_WT + w * 2048;
    *(LAS v4u*)(wt + lane * 16) = wqr[0]; *(LAS v4u*)(wt + 1024 + lane * 16) = wqr[1];
    *(LAS v4u*)(wt + 16384 + lane * 16) = w1r[0]; *(LAS v4u*)(wt + 16384 + 1024 + lane * 16) = w1r[1];
    asm volatile("s_waitcnt lgkmcnt(0)" ::: "memory");
    v4u xb[2];
#pragma unroll
    for (int t = 0; t < 2; ++t) {
      LAS unsigned char* xp = L + L_XN + (c & 7) * XN_STR + (64 * w + 32 * t + 4 * g) * 2;
      const v2u lo = *(LAS v2u*)xp, hi = *(LAS v2u*)(xp + 32);
      xb[t] = v4u{lo.x, lo.y, hi.x, hi.y};
    }
    v4f aq = {0.f, 0.f, 0.f, 0.f}, ah = {0.f, 0.f, 0.f, 0.f};
#pragma unroll
    for (int t = 0; t < 2; ++t) {
      const v2u q0 = trread(wt + t * 1024 + lane * 8), q1 = trread(wt + t * 1024 + 512 + lane * 8);
      aq = mfma32(v4u{q0.x, q0.y, q1.x, q1.y}, xb[t], aq);
      const v2u h0 = trread(wt + 16384 + t * 1024 + lane * 8), h1 = trread(wt + 16384 + t * 1024 + 512 + lane * 8);
      ah = mfma32(v4u{h0.x, h0.y, h1.x, h1.y}, xb[t], ah);
    }
    *(LAS v4f*)(L + L_RED1 + w * 1024 + lane * 16) = aq;
    *(LAS v4f*)(L + L_RED1 + 8192 + w * 1024 + lane * 16) = ah;
  }
  STAMP(3);
  BAR();
#define PIN(x) asm volatile("" : "+v"(x))
  KST(0); SB(); KLD(4); SB(); KST(1); SB(); KLD(5); SB(); KST(2); SB(); KLD(6); SB(); KST(3); SB(); KLD(7); SB();
  KST(4); SB(); VLD(0); SB(); KST(5); SB(); VLD(1); SB(); KST(6); SB(); VLD(2); SB(); KST(7); SB(); VLD(3); SB();
  PIN(V[0]); VLD(4); SB(); PIN(V[1]); VLD(5); SB(); PIN(V[2]); VLD(6); SB(); PIN(V[3]); VLD(7); SB();
  v4u wor[2], w2r[2];
  {
    const u32 vo = (hh * 128 + 16 * w + (lane >> 3)) * (DM * 2) + cs * 128 + (lane & 7) * 16;
    const u32 vo2 = ((ffn2 ? hh : 0) * 128 + 16 * w + (lane >> 3)) * (DM * 2) + cs * 128 + (lane & 7) * 16;
    wor[0] = LDNT128(rwo, vo, 0); wor[1] = LDNT128(rwo, vo, 8 * DM * 2);
    w2r[0] = LDNT128(rw2, vo2, 0); w2r[1] = LDNT128(rw2, vo2, 8 * DM * 2);
  }
  SB();
  const u32 tag = *(LAS u32*)(L + L_TAG);
  STAMP(4);
  BAR();
  STAMP(5);
  float sc[8];
  {
    v4u qf[4];
#pragma unroll
    for (int i = 0; i < 4; ++i) qf[i] = *(LAS v4u*)(L + L_QL + (c >> 2) * QL_STR + i * 64 + g * 16);
    v4f s0 = {0.f, 0.f, 0.f, 0.f}, s1 = s0;
#pragma unroll
    for (int i = 0; i < 4; ++i) {
      const v4u k0 = *(LAS v4u*)(kl + c * 256 + (((4 * i + g) ^ c) * 16)), k1 = *(LAS v4u*)(kl + 4096 + c * 256 + (((4 * i + g) ^ c) * 16));
      s0 = mfma32(k0, qf[i], s0); s1 = mfma32(k1, qf[i], s1);
    }
    constexpr float SCALE = 0.08838834764831845f;
#pragma unroll
    for (int r = 0; r < 4; r += 2) {
      const u32 p0 = cvtpk(s0[r], s0[r + 1]), p1 = cvtpk(s1[r], s1[r + 1]);
      const u32 t0 = cvtpk(bflo(p0) * SCALE, bfhi(p0) * SCALE), t1 = cvtpk(bflo(p1) * SCALE, bfhi(p1) * SCALE);
      sc[r] = bflo(t0); sc[r + 1] = bfhi(t0); sc[4 + r] = bflo(t1); sc[4 + r + 1] = bfhi(t1);
    }
  }
  float mx = fmaxf(fmaxf(fmaxf(sc[0], sc[1]), fmaxf(sc[2], sc[3])), fmaxf(fmaxf(sc[4], sc[5]), fmaxf(sc[6], sc[7])));
  mx = x16_max(mx);
  LAS float* mxl = (LAS float*)(L + L_MX);
  if (g == 0 && (c & 3) == 0) mxl[(c >> 2) * 8 + w] = mx;
  BAR();
  float M;
  {
    const v4f m0 = *(LAS v4f*)(mxl + (c >> 2) * 8), m1 = *(LAS v4f*)(mxl + (c >> 2) * 8 + 4);
    M = fmaxf(fmaxf(fmaxf(m0.x, m0.y), fmaxf(m0.z, m0.w)), fmaxf(fmaxf(m1.x, m1.y), fmaxf(m1.z, m1.w)));
  }
  v4u P;
  {
    constexpr float L2E = 1.4426950408889634f;
    float e[8], ls = 0.f;
#pragma unroll
    for (int i = 0; i < 8; ++i) { e[i] = __builtin_amdgcn_exp2f((sc[i] - M) * L2E); ls += e[i]; }
    P = v4u{cvtpk(e[0], e[1]), cvtpk(e[2], e[3]), cvtpk(e[4], e[5]), cvtpk(e[6], e[7])};
    ls = x16_sum(ls);
    if (g == 0 && (c & 3) == 0) mxl[32 + (c >> 2) * 8 + w] = ls;
  }
  STAMP(6);
  {
    v4f o[8];
#pragma unroll
    for (int jj = 0; jj < 8; ++jj) {
      const u32 sel = (jj & 1) ? 0x07060302u : 0x05040100u;
      v4u bf;
#pragma unroll
      for (int pr = 0; pr < 4; ++pr) bf[pr] = __builtin_amdgcn_perm(V[2 * pr + 1][jj >> 1], V[2 * pr][jj >> 1], sel);
      o[jj] = mfma32(P, bf, v4f{0.f, 0.f, 0.f, 0.f});
    }
    LAS unsigned char* r2 = L + L_WT + w * 2048 + g * 512 + c * 32;
    *(LAS v4f*)r2 = v4f{o[0][0], o[1][0], o[2][0], o[3][0]};
    *(LAS v4f*)(r2 + 16) = v4f{o[4][0], o[5][0], o[6][0], o[7][0]};
  }
  STAMP(7);
  BAR();
  if (tid < 128) {
    const int hd = tid >> 5, dq = tid & 31;
    LAS v4f* r2 = (LAS v4f*)(L + L_WT + tid * 16);
    v4f o = r2[0];
#pragma unroll
    for (int ww = 1; ww < 8; ++ww) o += r2[ww * 128];
    const v4f l0 = *(LAS v4f*)(mxl + 32 + hd * 8), l1 = *(LAS v4f*)(mxl + 32 + hd * 8 + 4);
    const float Ls = ((l0.x + l0.y) + (l0.z + l0.w)) + ((l1.x + l1.y) + (l1.z + l1.w));
    const float inv = __builtin_amdgcn_rcpf(Ls);
    const u32 h32 = 4 * hkv + hd;
    STSC128((v4u{cvtpk(o.x * inv, o.y * inv), tag, cvtpk(o.z * inv, o.w * inv), tag}), rws, WS_PO + ((h32 * 8 + b) * 4 + s) * 512 + dq * 16, 0);
  }
  if (w == 2 && lane < 4) {
    const v4f l0 = *(LAS v4f*)(mxl + 32 + lane * 8), l1 = *(LAS v4f*)(mxl + 32 + lane * 8 + 4);
    const float Ls = ((l0.x + l0.y) + (l0.z + l0.w)) + ((l1.x + l1.y) + (l1.z + l1.w));
    const v4f m0 = *(LAS v4f*)(mxl + lane * 8), m1 = *(LAS v4f*)(mxl + lane * 8 + 4);
    const float Mh = fmaxf(fmaxf(fmaxf(m0.x, m0.y), fmaxf(m0.z, m0.w)), fmaxf(fmaxf(m1.x, m1.y), fmaxf(m1.z, m1.w)));
    STSC128((v4u{__float_as_uint(Mh), tag, __float_as_uint(Ls), tag}), rws, WS_ST + ((hkv * 8 + b) * 4 + s) * 128 + lane * 16, 0);
  }
  STAMP(8);
  {
    LAS unsigned char* wl = L + L_K + w * 8192;
    *(LAS v4u*)(wl + (lane >> 3) * 160 + (lane & 7) * 16) = wor[0]; *(LAS v4u*)(wl + (8 + (lane >> 3)) * 160 + (lane & 7) * 16) = wor[1];
    *(LAS v4u*)(wl + 2560 + (lane >> 3) * 160 + (lane & 7) * 16) = w2r[0]; *(LAS v4u*)(wl + 2560 + (8 + (lane >> 3)) * 160 + (lane & 7) * 16) = w2r[1];
    asm volatile("s_waitcnt lgkmcnt(0)" ::: "memory");
    v4u wa[4];
#pragma unroll
    for (int ct = 0; ct < 4; ++ct) {
      LAS unsigned char* tp = wl + (4 * g + (c >> 2)) * 160 + ct * 32 + (c & 3) * 8;
      const v2u t0 = trread(tp), t1 = trread(tp + 2560);
      wa[ct] = v4u{t0.x, t0.y, t1.x, t1.y};
    }
    const u32 poff = WS_PO + ((hh * 8 + w) * 4) * 512 + lane * 16;
    const u32 soff = WS_ST + ((hkv * 8 + w) * 4 + (lane >> 5)) * 128 + (hh & 3) * 16;
    const u32 hoff = WS_HG + ((ffn2 ? hh : 0) * 8 + w) * 512 + g * 128 + (c & 7) * 16;
    v4u po[2], st[2], hg = {0u, tag, 0u, tag};
    for (u32 spin = 0; spin < SPIN_CAP; ++spin) {
      asm volatile("" ::: "memory");
#pragma unroll
      for (int i = 0; i < 2; ++i) { po[i] = LDSC128(rws, poff + i * 1024, 0); st[i] = LDSC128(rws, soff + i * 256, 0); }
      if (ffn2) hg = LDSC128(rws, hoff, 0);
      bool ok = (hg.y == tag) & (hg.w == tag);
#pragma unroll
      for (int i = 0; i < 2; ++i) ok &= (po[i].y == tag) & (po[i].w == tag) & (st[i].y == tag) & (st[i].w == tag);
      if (__all(ok)) break;
    }
    STAMP(9);
    float Mg = fmaxf(__uint_as_float(st[0].x), __uint_as_float(st[1].x));
    { auto t = __builtin_amdgcn_permlane32_swap(__float_as_uint(Mg), __float_as_uint(Mg), false, false); Mg = fmaxf(__uint_as_float(t[0]), __uint_as_float(t[1])); }
    float ws_[2];
#pragma unroll
    for (int i = 0; i < 2; ++i) ws_[i] = __builtin_amdgcn_exp2f((__uint_as_float(st[i].x) - Mg) * 1.4426950408889634f) * __uint_as_float(st[i].z);
    float W = ws_[0] + ws_[1];
    { auto t = __builtin_amdgcn_permlane32_swap(__float_as_uint(W), __float_as_uint(W), false, false); W = __uint_as_float(t[0]) + __uint_as_float(t[1]); }
    const float iw = __builtin_amdgcn_rcpf(W);
    const float f0 = ws_[0] * iw, f1 = ws_[1] * iw;
    float ov[4] = {fmaf(f0, bflo(po[0].x), f1 * bflo(po[1].x)), fmaf(f0, bfhi(po[0].x), f1 * bfhi(po[1].x)),
                   fmaf(f0, bflo(po[0].z), f1 * bflo(po[1].z)), fmaf(f0, bfhi(po[0].z), f1 * bfhi(po[1].z))};
#pragma unroll
    for (int i = 0; i < 4; ++i) { auto t = __builtin_amdgcn_permlane32_swap(__float_as_uint(ov[i]), __float_as_uint(ov[i]), false, false); ov[i] = __uint_as_float(t[0]) + __uint_as_float(t[1]); }
    if (lane < 32) *(LAS v2u*)(L + L_OA + w * OA_STR + lane * 8) = v2u{cvtpk(ov[0], ov[1]), cvtpk(ov[2], ov[3])};
    BAR();
    const v2u oa = *(LAS v2u*)(L + L_OA + (c & 7) * OA_STR + (16 * w + 4 * g) * 2);
    const v4u Bf = {oa.x, oa.y, hg.x, hg.z};
    LAS unsigned char* r3 = L + L_WT + 16384 + w * 2176;
#pragma unroll
    for (int ct = 0; ct < 4; ++ct) {
      const v4f d = mfma32(wa[ct], Bf, v4f{0.f, 0.f, 0.f, 0.f});
      if (c < 8) *(LAS v4f*)(r3 + c * 272 + ct * 64 + g * 16) = d;
    }
  }
  BAR();
  if (tid < 256) {
    LAS v2f* r3 = (LAS v2f*)(L + L_WT + 16384 + (tid >> 5) * 272 + (tid & 31) * 8);
    v2f o = r3[0];
#pragma unroll
    for (int ww = 1; ww < 8; ++ww) o += r3[ww * 272];
    const int pb = tid >> 5, col = cs * 64 + (tid & 31) * 2;
    const u32 off = WS_P2 + (((pb * 32 + (col >> 4)) * 32 + hh) * 16 + (col & 15)) * 8;
    STSC128((v4u{__float_as_uint(o.x), tag, __float_as_uint(o.y), tag}), rws, off, 0);
  }
  STAMP(10);
  if (w == 0) {
    const int fb = bid & 7, c16 = bid >> 3;
    const u32 xo = fb * 1024 + c16 * 32 + (lane & 7) * 4;
    const u32 xp = __builtin_amdgcn_raw_buffer_load_b32(rx, xo, 0, 0);
    const u32 off = WS_P2 + ((fb * 32 + c16) * 32 + (lane >> 3)) * 128 + (lane & 7) * 16;
    v4u p[4];
    for (u32 spin = 0; spin < SPIN_CAP; ++spin) {
      asm volatile("" ::: "memory");
#pragma unroll
      for (int i = 0; i < 4; ++i) p[i] = LDSC128(rws, off + i * 1024, 0);
      bool ok = true;
#pragma unroll
      for (int i = 0; i < 4; ++i) ok &= (p[i].y == tag) & (p[i].w == tag);
      if (__all(ok)) break;
    }
    float s0 = (__uint_as_float(p[0].x) + __uint_as_float(p[1].x)) + (__uint_as_float(p[2].x) + __uint_as_float(p[3].x));
    float s1 = (__uint_as_float(p[0].z) + __uint_as_float(p[1].z)) + (__uint_as_float(p[2].z) + __uint_as_float(p[3].z));
    s0 += dpp<ROR8>(s0); s1 += dpp<ROR8>(s1);
    s0 = x16_sum(s0); s1 = x16_sum(s1);
    if (lane < 8) ((GAS u32*)a.out)[(fb * 512 + c16 * 16) / 2 + lane] = cvtpk(bflo(xp) + s0, bfhi(xp) + s1);
    if (bid == 0 && lane == 0) __hip_atomic_store((GAS u32*)(a.ws + WS_EPOCH), tag, __ATOMIC_RELAXED, __HIP_MEMORY_SCOPE_AGENT);
  }
  if (bid == 0 && w == 0) { u64 t_; asm volatile("s_memrealtime %0\n\ts_waitcnt lgkmcnt(0)" : "=s"(t_) :: "memory");
    if (lane == 0) __hip_atomic_store((GAS u64*)(a.ws + 0x300000), t_, __ATOMIC_RELAXED, __HIP_MEMORY_SCOPE_AGENT); }
}
}

namespace fused {
__global__ void __launch_bounds__(64) dummy_k(unsigned long long* p) {
  u64 t_; asm volatile("s_memrealtime %0\n\ts_waitcnt lgkmcnt(0)" : "=s"(t_) :: "memory");
  const u64 td = __hip_atomic_load((GAS u64*)p, __ATOMIC_RELAXED, __HIP_MEMORY_SCOPE_AGENT);
  const u64 tdu = ((u64)__builtin_amdgcn_readfirstlane((u32)(td >> 32)) << 32) | (u32)__builtin_amdgcn_readfirstlane((u32)td);
  u64 d = t_ - tdu; if (d > 5000) d = 5000;
  u64 t2; asm volatile("s_memrealtime %0\n\ts_waitcnt lgkmcnt(0)" : "=s"(t2) :: "memory");
  const u64 until = t2 + 0 * d;
  for (int it = 0; it < 100000; ++it) { u64 n_; asm volatile("s_memrealtime %0\n\ts_waitcnt lgkmcnt(0)" : "=s"(n_) :: "memory"); if (n_ >= until) break; }
}
}
extern "C" void kernel_launch(void* const* d_in, const int* in_sizes, int n_in, void* d_out, int out_size, void* d_ws, size_t ws_size, hipStream_t stream) {
  using namespace fused;
  Args a;
  a.x = (const u16*)d_in[0]; a.kc = (const u16*)d_in[1]; a.vc = (const u16*)d_in[2]; a.wq = (const u16*)d_in[3]; a.wo = (const u16*)d_in[4];
  a.w1 = (const u16*)d_in[6]; a.w2 = (const u16*)d_in[7]; a.out = (u16*)d_out; a.ws = (unsigned char*)d_ws;
  hipLaunchKernelGGL(fused_decode, dim3(256), dim3(NTHR), 0, stream, a);
  hipLaunchKernelGGL(dummy_k, dim3(1), dim3(64), 0, stream, (unsigned long long*)((char*)d_ws + 0x300000));
}
```

```cpp
#include <hip/hip_runtime.h>
#include <hip/hip_bf16.h>

namespace fused {
typedef unsigned u32; typedef unsigned long long u64; typedef unsigned short u16;
typedef u32 v4u __attribute__((ext_vector_type(4)));
typedef u32 v2u __attribute__((ext_vector_type(2)));
typedef float v4f __attribute__((ext_vector_type(4)));
typedef float v2f __attribute__((ext_vector_type(2)));
typedef __bf16 bf8 __attribute__((ext_vector_type(8)));
typedef __bf16 bf2 __attribute__((ext_vector_type(2)));
#define LAS __attribute__((address_space(3)))
#define GAS __attribute__((address_space(1)))

constexpr int DM = 512, SK = 4096, DH = 128, WINK = 1024, FF = 1792, NQ = 4096;
constexpr int NAUX = 4, NSTR = 8, NWAVE = NAUX + 1 + NSTR, NTHR = NWAVE * 64;
constexpr int AUX_NT = 2, AUX_SC1 = 16;
constexpr u32 RSRC3 = 0x00020000u;
constexpr u32 SPIN_CAP = 1u << 16;
#define KW 3
#define QDELAY 24
#define QGAP 12

constexpr u32 WS_EPOCH = 0;
constexpr u32 WS_QG = 0x1000;
constexpr u32 WS_HQ = 0x81000;
constexpr u32 WS_HG = 0xC0000;
constexpr u32 WS_ST = 0xD0000;
constexpr u32 WS_PO = 0xE0000;
constexpr u32 WS_P2 = 0x160000;
constexpr u32 WS_END = 0x260000;
constexpr u32 WS_STAMP = 0x300000;

constexpr int L_CNT = 0;
constexpr int C_X = 0, C_P1 = 1, C_Q = 2, C_V = 3, C_QK = 4, C_TAG = 5, C_P2 = 6;
constexpr int L_MX = 64;
constexpr int L_QL = L_MX + 256, QL_STR = 320;
constexpr int L_OA = L_QL + 1280, OA_STR = 272;
constexpr int L_HA = L_OA + 8 * OA_STR;
constexpr int L_BN = L_HA + 8 * OA_STR;
constexpr int L_XN = L_BN + 2048 + 1536, XN_STR = 1040;
constexpr int L_RED1 = L_XN + 8 * XN_STR;
constexpr int L_WT = L_RED1 + 16384;
constexpr int L_K = L_WT + 32768;
constexpr int L_BYTES = L_K + 65536;
static_assert(L_XN % 16 == 0 && L_RED1 % 16 == 0 && L_WT % 16 == 0 && L_K % 16 == 0 && L_BYTES <= 163840 && L_BYTES > 81920, "LDS map");

__device__ __forceinline__ u32 cvtpk(float lo, float hi) { return __builtin_bit_cast(u32, __builtin_convertvector((v2f{lo, hi}), bf2)); }
__device__ __forceinline__ float bflo(u32 p) { return __builtin_bit_cast(float, p << 16); }
__device__ __forceinline__ float bfhi(u32 p) { return __builtin_bit_cast(float, p & 0xffff0000u); }
__device__ __forceinline__ float rbf(float x) { return bflo(cvtpk(x, x)); }
template <int CTRL> __device__ __forceinline__ float dpp(float x) {
  return __builtin_bit_cast(float, __builtin_amdgcn_mov_dpp(__builtin_bit_cast(int, x), CTRL, 0xf, 0xf, true));
}
constexpr int XOR1 = 0xB1, XOR2 = 0x4E, HMIR = 0x141, RMIR = 0x140, ROR8 = 0x128;
__device__ __forceinline__ float x32_sum(float x) {
  auto t = __builtin_amdgcn_permlane32_swap(__float_as_uint(x), __float_as_uint(x), false, false);
  return __uint_as_float(t[0]) + __uint_as_float(t[1]);
}
__device__ __forceinline__ float x32_max(float x) {
  auto t = __builtin_amdgcn_permlane32_swap(__float_as_uint(x), __float_as_uint(x), false, false);
  return fmaxf(__uint_as_float(t[0]), __uint_as_float(t[1]));
}
__device__ __forceinline__ float x16_sum(float x) {
  auto s = __builtin_amdgcn_permlane16_swap(__float_as_uint(x), __float_as_uint(x), false, false);
  return x32_sum(__uint_as_float(s[0]) + __uint_as_float(s[1]));
}
__device__ __forceinline__ float x16_max(float x) {
  auto s = __builtin_amdgcn_permlane16_swap(__float_as_uint(x), __float_as_uint(x), false, false);
  return x32_max(fmaxf(__uint_as_float(s[0]), __uint_as_float(s[1])));
}
__device__ __forceinline__ float half_sum(float x) {
  x += dpp<XOR1>(x); x += dpp<XOR2>(x); x += dpp<HMIR>(x); x += dpp<RMIR>(x);
  auto s = __builtin_amdgcn_permlane16_swap(__float_as_uint(x), __float_as_uint(x), false, false);
  return __uint_as_float(s[0]) + __uint_as_float(s[1]);
}
__device__ __forceinline__ float wave_sum(float x) {
  x += dpp<XOR1>(x); x += dpp<XOR2>(x); x += dpp<HMIR>(x); x += dpp<RMIR>(x); return x16_sum(x);
}
__device__ __forceinline__ v2u pack24(float a_, float b_, u32 tag16) {
  const u32 ua = __float_as_uint(a_) + 0x80u, ub = __float_as_uint(b_) + 0x80u;
  return v2u{__builtin_amdgcn_perm(ub, ua, 0x05030201u), __builtin_amdgcn_perm(tag16, ub, 0x05040302u)};
}
__device__ __forceinline__ float un24a(u32 d0) { return __uint_as_float(d0 << 8); }
__device__ __forceinline__ float un24b(u32 d0, u32 d1) { return __uint_as_float(__builtin_amdgcn_perm(d1, d0, 0x0504030Cu)); }
__device__ __forceinline__ v4f mfma32(v4u a, v4u b, v4f c) {
  return __builtin_amdgcn_mfma_f32_16x16x32_bf16(__builtin_bit_cast(bf8, a), __builtin_bit_cast(bf8, b), c, 0, 0, 0);
}
__device__ __forceinline__ v2u trread(LAS unsigned char* p) {
  typedef short v4s __attribute__((ext_vector_type(4)));
  return __builtin_bit_cast(v2u, __builtin_amdgcn_ds_read_tr16_b64_v4i16((LAS v4s*)p));
}
#define BAR() do { asm volatile("s_waitcnt lgkmcnt(0)" ::: "memory"); __builtin_amdgcn_s_barrier(); asm volatile("" ::: "memory"); } while (0)
#define SB() __builtin_amdgcn_sched_barrier(0)
#define LGKM0() asm volatile("s_waitcnt lgkmcnt(0)" ::: "memory")
#define PIN(x) asm volatile("" : "+v"(x))
__device__ __forceinline__ __amdgpu_buffer_rsrc_t mkrs(const void* p, u32 bytes) { return __builtin_amdgcn_make_buffer_rsrc((void*)p, 0, (int)bytes, RSRC3); }
#define LDNT128(r, vo, so) __builtin_amdgcn_raw_buffer_load_b128((r), (vo), (so), AUX_NT)
#define AUX_W 2
#define LDW128(r, vo, so) __builtin_amdgcn_raw_buffer_load_b128((r), (vo), (so), AUX_W)
#define LDSC128(r, vo, so) __builtin_amdgcn_raw_buffer_load_b128((r), (vo), (so), AUX_SC1)
#define STSC128(v, r, vo, so) __builtin_amdgcn_raw_buffer_store_b128((v), (r), (vo), (so), AUX_SC1)
__device__ __forceinline__ u32 cnt_add(LAS unsigned char* L, int c) {
  asm volatile("" ::: "memory");
  return __hip_atomic_fetch_add((LAS u32*)(L + L_CNT) + c, 1u, __ATOMIC_RELAXED, __HIP_MEMORY_SCOPE_WORKGROUP);
}
__device__ __forceinline__ void cnt_wait(LAS unsigned char* L, int c, u32 n) {
#pragma unroll 1
  for (u32 spin = 0; spin < (1u << 22); ++spin) {
    if (__hip_atomic_load((LAS u32*)(L + L_CNT) + c, __ATOMIC_RELAXED, __HIP_MEMORY_SCOPE_WORKGROUP) >= n) break;
    __builtin_amdgcn_s_sleep(1);
  }
  asm volatile("" ::: "memory");
}

#define STAMP(i) do { } while (0)

struct Args { const u16* x; const u16* kc; const u16* vc; const u16* wq; const u16* wo; const u16* w1; const u16* w2; u16* out; unsigned char* ws; };

__global__ void __launch_bounds__(NTHR, 4) fused_decode(Args a) {
  __shared__ __attribute__((aligned(16))) unsigned char smem[L_BYTES];
  LAS unsigned char* L = (LAS unsigned char*)smem;
  const int tid = threadIdx.x, lane = tid & 63, wv = __builtin_amdgcn_readfirstlane(tid >> 6);
  if (tid < 8) ((LAS u32*)(L + L_CNT))[tid] = 0u;
  const int bid = blockIdx.x, hkv = bid & 7, j = bid >> 3;
  const int g = lane >> 4, c = lane & 15;
  asm volatile("" :: "s"(a.x), "s"(a.kc), "s"(a.vc), "s"(a.wq), "s"(a.wo), "s"(a.w1), "s"(a.w2), "s"(a.out), "s"(a.ws));
  BAR();
  STAMP(0);
  const int b = j >> 2, s = j & 3;

  const __amdgpu_buffer_rsrc_t rx = mkrs(a.x, 8192);
  if (wv > NAUX) {
    const int w = wv - NAUX - 1;
    const size_t kvbase = ((size_t)(b * 8 + hkv) * SK + (SK - WINK) + s * 256 + w * 32) * DH * 2;
    const __amdgpu_buffer_rsrc_t rk = mkrs((const unsigned char*)a.kc + kvbase, 8192);
    const __amdgpu_buffer_rsrc_t rv = mkrs((const unsigned char*)a.vc + kvbase, 8192);
    v4u K[KW], V[8];
    const u32 vvo = g * 1024 + c * 16;
    const u32 kvo = g * 256 + c * 16;
    LAS unsigned char* kl = L + L_K + w * 8192;
#define KLD(u) K[(u) % KW] = LDNT128(rk, kvo + ((u) & 3) * 1024, ((u) >> 2) * 4096)
#define KST(u) *(LAS v4u*)(kl + (4 * (u) + g) * 256 + ((c ^ ((4 * (u) + g) & 15)) * 16)) = K[(u) % KW]
#define VLD(i) V[i] = LDNT128(rv, vvo + ((i) & 3) * 256, ((i) >> 2) * 4096)
    SB(); __builtin_amdgcn_s_barrier(); SB();
#pragma unroll
    for (int u = 0; u < KW; ++u) KLD(u);
    SB();
#pragma unroll
    for (int n = 0; n < 16 - KW; ++n) {
      if (n < 8) { KST(n); } else { PIN(V[n - 8]); }
      SB();
      if (n + KW < 8) { KLD(n + KW); } else { VLD(n + KW - 8); }
      SB();
      if (n == 7) STAMP(2);
    }
    if (lane == 0) cnt_add(L, C_V);
#pragma unroll
    for (int n = 16 - KW; n < 8; ++n) { KST(n); }
    STAMP(3);
    cnt_wait(L, C_Q, 1);
    STAMP(4);
    float sc[8];
    {
      v4u qf[4];
#pragma unroll
      for (int i = 0; i < 4; ++i) qf[i] = *(LAS v4u*)(L + L_QL + (c >> 2) * QL_STR + i * 64 + g * 16);
      v4f s0 = {0.f, 0.f, 0.f, 0.f}, s1 = s0;
#pragma unroll
      for (int i = 0; i < 4; ++i) {
        const v4u k0 = *(LAS v4u*)(kl + c * 256 + (((4 * i + g) ^ c) * 16)), k1 = *(LAS v4u*)(kl + 4096 + c * 256 + (((4 * i + g) ^ c) * 16));
        s0 = mfma32(k0, qf[i], s0); s1 = mfma32(k1, qf[i], s1);
      }
      constexpr float SCALE = 0.08838834764831845f;
#pragma unroll
      for (int r = 0; r < 4; r += 2) {
        const u32 p0 = cvtpk(s0[r], s0[r + 1]), p1 = cvtpk(s1[r], s1[r + 1]);
        const u32 t0 = cvtpk(bflo(p0) * SCALE, bfhi(p0) * SCALE), t1 = cvtpk(bflo(p1) * SCALE, bfhi(p1) * SCALE);
        sc[r] = bflo(t0); sc[r + 1] = bfhi(t0); sc[4 + r] = bflo(t1); sc[4 + r + 1] = bfhi(t1);
      }
    }
    float mx = fmaxf(fmaxf(fmaxf(sc[0], sc[1]), fmaxf(sc[2], sc[3])), fmaxf(fmaxf(sc[4], sc[5]), fmaxf(sc[6], sc[7])));
    mx = x16_max(mx);
    v4u P;
    {
      constexpr float L2E = 1.4426950408889634f;
      float e[8], ls = 0.f;
#pragma unroll
      for (int i = 0; i < 8; ++i) { e[i] = __builtin_amdgcn_exp2f((sc[i] - mx) * L2E); ls += e[i]; }
      P = v4u{cvtpk(e[0], e[1]), cvtpk(e[2], e[3]), cvtpk(e[4], e[5]), cvtpk(e[6], e[7])};
      ls = x16_sum(ls);
      LAS float* mxl = (LAS float*)(L + L_MX);
      if (g == 0 && (c & 3) == 0) { mxl[(c >> 2) * 8 + w] = mx; mxl[32 + (c >> 2) * 8 + w] = ls; }
      if (lane == 0) cnt_add(L, C_QK);
    }
    STAMP(5);
    {
      v4f o[8];
#pragma unroll
      for (int jj = 0; jj < 8; ++jj) {
        const u32 sel = (jj & 1) ? 0x07060302u : 0x05040100u;
        v4u bf;
#pragma unroll
        for (int pr = 0; pr < 4; ++pr) bf[pr] = __builtin_amdgcn_perm(V[2 * pr + 1][jj >> 1], V[2 * pr][jj >> 1], sel);
        o[jj] = mfma32(P, bf, v4f{0.f, 0.f, 0.f, 0.f});
      }
      LAS unsigned char* r2 = L + L_WT + w * 2048 + g * 512 + c * 32;
      *(LAS v4f*)r2 = v4f{o[0][0], o[1][0], o[2][0], o[3][0]};
      *(LAS v4f*)(r2 + 16) = v4f{o[4][0], o[5][0], o[6][0], o[7][0]};
    }
    STAMP(6);
    LGKM0();
    return;
  }

  const int hh = 4 * hkv + (j & 3), cs = j >> 2;
  const int t1 = (hkv - 4) * 8 + cs;
  const bool ffn2 = hh < 14, ffn1 = hkv >= 4 && t1 < 28;
  const __amdgpu_buffer_rsrc_t rws = mkrs(a.ws, WS_END);
  __builtin_amdgcn_s_setprio(2);
  if (wv == NAUX) {
    const u32 ep = __builtin_amdgcn_raw_buffer_load_b32(rws, WS_EPOCH, 0, AUX_SC1);
    SB(); __builtin_amdgcn_s_barrier(); SB();
    const u32 tag = __builtin_amdgcn_readfirstlane(ep) + 1u;
    if (lane == 0) ((LAS u32*)(L + L_CNT))[C_TAG] = tag;
    const u32 tg16 = tag & 0xffffu;
    const int kq = j & 3;
    cnt_wait(L, C_P1, NAUX);
    STAMP(2);
#pragma unroll
    for (int tl = 0; tl < 2; ++tl) {
      if (tl == 1 && !ffn1) break;
#pragma unroll
      for (int s2 = 0; s2 < 2; ++s2) {
        const int pb = 4 * s2 + (lane >> 4);
        LAS v4f* red = (LAS v4f*)(L + L_RED1 + tl * 8192 + ((lane & 15) >> 2) * 512 + (lane & 3) * 128 + pb * 16);
        const v4f sv = (red[0] + red[128]) + (red[256] + red[384]);
        const v2u g0 = pack24(sv.x, sv.y, tg16), g1 = pack24(sv.z, sv.w, tg16);
        const u32 dst = tl == 0 ? WS_QG + (((pb * 8 + hkv) * 4 + kq) * 8 + cs) * 256 : WS_HQ + ((pb * 28 + t1) * 4 + kq) * 256;
        STSC128((v4u{g0.x, g0.y, g1.x, g1.y}), rws, dst + (lane & 15) * 16, 0);
      }
    }
    STAMP(3);
    __builtin_amdgcn_s_sleep(QDELAY);
#define T16OK(x) ((((x).y >> 16) == tg16) & (((x).w >> 16) == tg16))
    {
      const u32 off = WS_QG + (b * 8 + hkv) * 8192 + lane * 16;
      v4u qp[8];
#pragma unroll 1
      for (u32 spin = 0; spin < SPIN_CAP; ++spin) {
        asm volatile("" ::: "memory");
#pragma unroll
        for (int i = 0; i < 8; ++i) qp[i] = LDSC128(rws, off + (i >> 1) * 2048 + (i & 1) * 1024, 0);
        bool ok = true;
#pragma unroll
        for (int i = 0; i < 8; ++i) ok &= T16OK(qp[i]);
        if (__all(ok)) break;
      }
#pragma unroll
      for (int hf = 0; hf < 2; ++hf) {
        float v0 = 0.f, v1 = 0.f, v2 = 0.f, v3 = 0.f;
#pragma unroll
        for (int k4 = 0; k4 < 4; ++k4) { const v4u p = qp[2 * k4 + hf]; v0 += un24a(p.x); v1 += un24b(p.x, p.y); v2 += un24a(p.z); v3 += un24b(p.z, p.w); }
        *(LAS v2u*)(L + L_QL + (2 * hf + (lane >> 5)) * QL_STR + (lane & 31) * 8) = v2u{cvtpk(v0, v1), cvtpk(v2, v3)};
      }
      if (lane == 0) cnt_add(L, C_Q);
    }
    STAMP(4);
    if (ffn1) {
      const u32 off = WS_HQ + (((2 * kq + ((lane >> 4) & 1)) * 28 + t1) * 4 + 2 * (lane >> 5)) * 256 + (lane & 15) * 16;
      v4u h0, h1;
#pragma unroll 1
      for (u32 spin = 0; spin < SPIN_CAP; ++spin) {
        asm volatile("" ::: "memory");
        h0 = LDSC128(rws, off, 0); h1 = LDSC128(rws, off + 256, 0);
        if (__all(T16OK(h0) & T16OK(h1))) break;
        __builtin_amdgcn_s_sleep(8);
      }
      float hv[4] = {x32_sum(un24a(h0.x) + un24a(h1.x)), x32_sum(un24b(h0.x, h0.y) + un24b(h1.x, h1.y)),
                     x32_sum(un24a(h0.z) + un24a(h1.z)), x32_sum(un24b(h0.z, h0.w) + un24b(h1.z, h1.w))};
#pragma unroll
      for (int r = 0; r < 4; ++r) { const float hb = rbf(hv[r]); hv[r] = hb * __builtin_amdgcn_rcpf(1.0f + __builtin_amdgcn_exp2f(-1.4426950408889634f * hb)); }
      if (lane < 32) STSC128((v4u{cvtpk(hv[0], hv[1]), tag, cvtpk(hv[2], hv[3]), tag}), rws, WS_HG + (t1 * 8 + 2 * kq + (lane >> 4)) * 256 + (lane & 15) * 16, 0);
    } else if (ffn2) {
      const u32 off = WS_HG + hh * 4096 + lane * 16;
      v4u hv[4];
#pragma unroll 1
      for (u32 spin = 0; spin < SPIN_CAP; ++spin) {
        asm volatile("" ::: "memory");
        bool ok = true;
#pragma unroll
        for (int i = 0; i < 4; ++i) hv[i] = LDSC128(rws, off + i * 1024, 0);
#pragma unroll
        for (int i = 0; i < 4; ++i) ok &= (hv[i].y == tag) & (hv[i].w == tag);
        if (__all(ok)) break;
        __builtin_amdgcn_s_sleep(16);
      }
#pragma unroll
      for (int i = 0; i < 4; ++i)
        *(LAS v2u*)(L + L_HA + (4 * (i & 1) + (lane >> 4)) * OA_STR + (64 * (i >> 1) + 4 * (lane & 15)) * 2) = v2u{hv[i].x, hv[i].z};
    }
    STAMP(5);
    LGKM0();
    return;
  }

  const int w = wv;
  v4u xr[2], wr[4], w1r[4];
  {
    const u32 xo = (2 * w + (lane >> 5)) * 1024 + (lane & 31) * 16;
    xr[0] = __builtin_amdgcn_raw_buffer_load_b128(rx, xo, 0, 0);
    xr[1] = __builtin_amdgcn_raw_buffer_load_b128(rx, xo + 512, 0, 0);
    const __amdgpu_buffer_rsrc_t rwq = mkrs(a.wq, DM * NQ * 2);
    const u32 vo = (128 * (j & 3) + 32 * w + (lane >> 3)) * (NQ * 2) + (hkv * 512 + cs * 64) * 2 + (lane & 7) * 16;
#pragma unroll
    for (int u = 0; u < 4; ++u) wr[u] = LDW128(rwq, vo, u * 8 * NQ * 2);
    const __amdgpu_buffer_rsrc_t rw1 = mkrs(a.w1, ffn1 ? DM * FF * 2 : 0);
    const u32 vo1 = (128 * (j & 3) + 32 * w + (lane >> 3)) * (FF * 2) + (ffn1 ? t1 : 0) * 128 + (lane & 7) * 16;
#pragma unroll
    for (int u = 0; u < 4; ++u) w1r[u] = LDW128(rw1, vo1, u * 8 * FF * 2);
  }
  SB(); __builtin_amdgcn_s_barrier(); SB();
  {
    float xv[16];
#pragma unroll
    for (int h2 = 0; h2 < 2; ++h2)
#pragma unroll
      for (int i = 0; i < 4; ++i) { xv[8 * h2 + 2 * i] = bflo(xr[h2][i]); xv[8 * h2 + 2 * i + 1] = bfhi(xr[h2][i]); }
    float sm = (((xv[0] + xv[1]) + (xv[2] + xv[3])) + ((xv[4] + xv[5]) + (xv[6] + xv[7]))) + (((xv[8] + xv[9]) + (xv[10] + xv[11])) + ((xv[12] + xv[13]) + (xv[14] + xv[15])));
    sm = half_sum(sm);
    const float mean = sm * (1.0f / DM);
    float s0 = 0.f, s1 = 0.f;
#pragma unroll
    for (int i = 0; i < 8; ++i) { const float d0 = xv[i] - mean, d1 = xv[8 + i] - mean; s0 = fmaf(d0, d0, s0); s1 = fmaf(d1, d1, s1); }
    const float ss = half_sum(s0 + s1);
    const float mb = rbf(mean), var = rbf(ss * (1.0f / DM));
    const float den = rbf(__builtin_amdgcn_sqrtf(rbf(var + 1e-5f)));
    const float inv = __builtin_amdgcn_rcpf(den);
#pragma unroll
    for (int h2 = 0; h2 < 2; ++h2) {
      v4u o;
#pragma unroll
      for (int i = 0; i < 4; ++i) {
        const u32 dd = cvtpk(xv[8 * h2 + 2 * i] - mb, xv[8 * h2 + 2 * i + 1] - mb);
        o[i] = cvtpk(bflo(dd) * inv, bfhi(dd) * inv);
      }
      *(LAS v4u*)(L + L_XN + (2 * w + (lane >> 5)) * XN_STR + h2 * 512 + (lane & 31) * 16) = o;
    }
    if (lane == 0) cnt_add(L, C_X);
  }
  STAMP(1);
  {
    LAS unsigned char* wt = L + L_WT + w * 8192;
    const int wst = (lane >> 3) * 128 + (((lane & 7) ^ ((lane >> 3) & 6)) * 16);
    const int wf = (2 * g + (c >> 3)) & 3;
    const int wrd = (4 * g + (c >> 2)) * 128 + (c & 1) * 8;
#pragma unroll
    for (int u = 0; u < 4; ++u) *(LAS v4u*)(wt + u * 1024 + wst) = wr[u];
    asm volatile("" ::: "memory");
    v4u wa1[4];
#pragma unroll
    for (int ct = 0; ct < 4; ++ct) {
      LAS unsigned char* tp = wt + wrd + (((2 * ct + ((c & 3) >> 1)) ^ (2 * wf)) * 16);
      const v2u p0 = trread(tp), p1 = trread(tp + 2048);
      wa1[ct] = v4u{p0.x, p0.y, p1.x, p1.y};
    }
    PIN(wa1[0]); PIN(wa1[1]); PIN(wa1[2]); PIN(wa1[3]);
    cnt_wait(L, C_X, NAUX);
    LAS unsigned char* xp = L + L_XN + (c & 7) * XN_STR + (128 * (j & 3) + 32 * w + 4 * g) * 2;
    const v2u lo = *(LAS v2u*)xp, hi = *(LAS v2u*)(xp + 32);
    const v4u xb = v4u{lo.x, lo.y, hi.x, hi.y};
#pragma unroll
    for (int ct = 0; ct < 4; ++ct) {
      const v4f d = mfma32(wa1[ct], xb, v4f{0.f, 0.f, 0.f, 0.f});
      if (c < 8) *(LAS v4f*)(L + L_RED1 + w * 2048 + ct * 512 + g * 128 + c * 16) = d;
    }
    if (ffn1) {
#pragma unroll
      for (int u = 0; u < 4; ++u) *(LAS v4u*)(wt + 4096 + u * 1024 + wst) = w1r[u];
      asm volatile("" ::: "memory");
#pragma unroll
      for (int ct = 0; ct < 4; ++ct) {
        LAS unsigned char* tp = wt + 4096 + wrd + (((2 * ct + ((c & 3) >> 1)) ^ (2 * wf)) * 16);
        const v2u p0 = trread(tp), p1 = trread(tp + 2048);
        const v4f d = mfma32(v4u{p0.x, p0.y, p1.x, p1.y}, xb, v4f{0.f, 0.f, 0.f, 0.f});
        if (c < 8) *(LAS v4f*)(L + L_RED1 + 8192 + w * 2048 + ct * 512 + g * 128 + c * 16) = d;
      }
    }
    if (lane == 0) cnt_add(L, C_P1);
  }
  STAMP(2);
  STAMP(3);
  cnt_wait(L, C_V, NSTR);
  STAMP(4);
  v4u wor[4], w2r[4];
  {
    const __amdgpu_buffer_rsrc_t rwo = mkrs(a.wo, NQ * DM * 2);
    const __amdgpu_buffer_rsrc_t rw2 = mkrs(a.w2, ffn2 ? FF * DM * 2 : 0);
    const u32 vo = (hh * 128 + 32 * w + (lane >> 3)) * (DM * 2) + cs * 128 + (lane & 7) * 16;
    const u32 vo2 = ((ffn2 ? hh : 0) * 128 + 32 * w + (lane >> 3)) * (DM * 2) + cs * 128 + (lane & 7) * 16;
#pragma unroll
    for (int u = 0; u < 4; ++u) wor[u] = LDNT128(rwo, vo, u * 8 * DM * 2);
#pragma unroll
    for (int u = 0; u < 4; ++u) w2r[u] = LDNT128(rw2, vo2, u * 8 * DM * 2);
  }
  SB();
  STAMP(5);
  cnt_wait(L, C_QK, NSTR);
  const u32 tag = ((LAS u32*)(L + L_CNT))[C_TAG];
  v4f fw;
  {
    LAS float* mxl = (LAS float*)(L + L_MX);
    const v4f m0 = *(LAS v4f*)(mxl + w * 8), m1 = *(LAS v4f*)(mxl + w * 8 + 4);
    const v4f l0 = *(LAS v4f*)(mxl + 32 + w * 8), l1 = *(LAS v4f*)(mxl + 32 + w * 8 + 4);
    const float M = fmaxf(fmaxf(fmaxf(m0.x, m0.y), fmaxf(m0.z, m0.w)), fmaxf(fmaxf(m1.x, m1.y), fmaxf(m1.z, m1.w)));
    constexpr float L2E = 1.4426950408889634f;
    v4f f0, f1;
#pragma unroll
    for (int i = 0; i < 4; ++i) { f0[i] = __builtin_amdgcn_exp2f((m0[i] - M) * L2E); f1[i] = __builtin_amdgcn_exp2f((m1[i] - M) * L2E); }
    float Ls = 0.f;
#pragma unroll
    for (int i = 0; i < 4; ++i) { Ls = fmaf(f0[i], l0[i], Ls); Ls = fmaf(f1[i], l1[i], Ls); }
    const float inv = __builtin_amdgcn_rcpf(Ls);
    const bool lo = lane < 32;
    fw = v4f{lo ? f0.x : f1.x, lo ? f0.y : f1.y, lo ? f0.z : f1.z, lo ? f0.w : f1.w} * inv;
    if (w == 0) {
      const int hd = lane & 3;
      const v4f a0 = *(LAS v4f*)(mxl + hd * 8), a1 = *(LAS v4f*)(mxl + hd * 8 + 4);
      const v4f b0 = *(LAS v4f*)(mxl + 32 + hd * 8), b1 = *(LAS v4f*)(mxl + 32 + hd * 8 + 4);
      const float Mh = fmaxf(fmaxf(fmaxf(a0.x, a0.y), fmaxf(a0.z, a0.w)), fmaxf(fmaxf(a1.x, a1.y), fmaxf(a1.z, a1.w)));
      float Lh = 0.f;
#pragma unroll
      for (int i = 0; i < 4; ++i) { Lh = fmaf(__builtin_amdgcn_exp2f((a0[i] - Mh) * L2E), b0[i], Lh); Lh = fmaf(__builtin_amdgcn_exp2f((a1[i] - Mh) * L2E), b1[i], Lh); }
      if (lane < 4) STSC128((v4u{__float_as_uint(Mh), tag, __float_as_uint(Lh), tag}), rws, WS_ST + ((hkv * 8 + b) * 4 + s) * 128 + lane * 16, 0);
    }
  }
  STAMP(6);
  BAR();
  STAMP(7);
  {
    LAS v4f* r2 = (LAS v4f*)(L + L_WT + (lane >> 5) * 8192 + w * 512 + (lane & 31) * 16);
    v4f o = r2[0] * fw[0];
    o += r2[128] * fw[1]; o += r2[256] * fw[2]; o += r2[384] * fw[3];
    o.x = x32_sum(o.x); o.y = x32_sum(o.y); o.z = x32_sum(o.z); o.w = x32_sum(o.w);
    if (lane < 32) STSC128((v4u{cvtpk(o.x, o.y), tag, cvtpk(o.z, o.w), tag}), rws, WS_PO + (((4 * hkv + w) * 8 + b) * 4 + s) * 512 + lane * 16, 0);
  }
  STAMP(8);
  v4u wa[8];
  {
    LAS unsigned char* wl = L + L_K;
#pragma unroll
    for (int u = 0; u < 4; ++u) {
      *(LAS v4u*)(wl + (32 * w + 8 * u + (lane >> 3)) * 160 + (lane & 7) * 16) = wor[u];
      *(LAS v4u*)(wl + 20480 + (32 * w + 8 * u + (lane >> 3)) * 160 + (lane & 7) * 16) = w2r[u];
    }
    BAR();
#pragma unroll
    for (int t = 0; t < 4; ++t) {
      LAS unsigned char* tp = wl + (32 * t + 4 * g + (c >> 2)) * 160 + w * 32 + (c & 3) * 8;
      const v2u t0 = trread(tp), t1 = trread(tp + 16 * 160), t2 = trread(tp + 20480), t3 = trread(tp + 20480 + 16 * 160);
      wa[t] = v4u{t0.x, t0.y, t1.x, t1.y};
      wa[4 + t] = v4u{t2.x, t2.y, t3.x, t3.y};
    }
  }
  {
    const u32 poff = WS_PO + ((hh * 8 + 2 * w) * 4) * 512 + lane * 16;
    const u32 soff = WS_ST + ((hkv * 8 + 2 * w) * 4 + (lane >> 5)) * 128 + (hh & 3) * 16;
    v4u po[4], st[4];
#pragma unroll 1
    for (u32 spin = 0; spin < SPIN_CAP; ++spin) {
      asm volatile("" ::: "memory");
      bool ok = true;
#pragma unroll
      for (int i = 0; i < 4; ++i) { po[i] = LDSC128(rws, poff + i * 1024, 0); st[i] = LDSC128(rws, soff + i * 256, 0); }
#pragma unroll
      for (int i = 0; i < 4; ++i) ok &= (po[i].y == tag) & (po[i].w == tag) & (st[i].y == tag) & (st[i].w == tag);
      if (__all(ok)) break;
    }
    STAMP(9);
#pragma unroll
    for (int bb = 0; bb < 2; ++bb) {
      const v4u s0 = st[2 * bb], s1 = st[2 * bb + 1], p0 = po[2 * bb], p1 = po[2 * bb + 1];
      const float Mg = x32_max(fmaxf(__uint_as_float(s0.x), __uint_as_float(s1.x)));
      const float w0 = __builtin_amdgcn_exp2f((__uint_as_float(s0.x) - Mg) * 1.4426950408889634f) * __uint_as_float(s0.z);
      const float w1 = __builtin_amdgcn_exp2f((__uint_as_float(s1.x) - Mg) * 1.4426950408889634f) * __uint_as_float(s1.z);
      const float iw = __builtin_amdgcn_rcpf(x32_sum(w0 + w1));
      const float f0 = w0 * iw, f1 = w1 * iw;
      const float o0 = x32_sum(fmaf(f0, bflo(p0.x), f1 * bflo(p1.x))), o1 = x32_sum(fmaf(f0, bfhi(p0.x), f1 * bfhi(p1.x)));
      const float o2 = x32_sum(fmaf(f0, bflo(p0.z), f1 * bflo(p1.z))), o3 = x32_sum(fmaf(f0, bfhi(p0.z), f1 * bfhi(p1.z)));
      if (lane < 32) *(LAS v2u*)(L + L_OA + (2 * w + bb) * OA_STR + lane * 8) = v2u{cvtpk(o0, o1), cvtpk(o2, o3)};
    }
  }
  BAR();
  {
    v4f d0 = {0.f, 0.f, 0.f, 0.f}, d1 = d0;
#pragma unroll
    for (int t = 0; t < 4; ++t) {
      LAS unsigned char* op = L + L_OA + (c & 7) * OA_STR + (32 * t + 4 * g) * 2;
      const v2u lo = *(LAS v2u*)op, hi = *(LAS v2u*)(op + 32);
      d0 = mfma32(wa[t], v4u{lo.x, lo.y, hi.x, hi.y}, d0);
      if (ffn2) {
        const v2u hl = *(LAS v2u*)(op + 8 * OA_STR), hh_ = *(LAS v2u*)(op + 8 * OA_STR + 32);
        d1 = mfma32(wa[4 + t], v4u{hl.x, hl.y, hh_.x, hh_.y}, d1);
      }
    }
    d0 += d1;
    LAS unsigned char* bn = L + L_BN + w * 512;
    if (c < 8) *(LAS v4f*)(bn + c * 64 + g * 16) = d0;
    LGKM0();
    const v2f pv = *(LAS v2f*)(bn + lane * 8);
    STSC128((v4u{__float_as_uint(pv.x), tag, __float_as_uint(pv.y), tag}), rws,
            WS_P2 + ((((lane >> 3) * 32 + cs * 4 + w) * 32 + hh) * 16 + (lane & 7) * 2) * 8, 0);
  }
  STAMP(10);
  if (w == 0) {
    const int fb = bid & 7, c16 = bid >> 3;
    const u32 xo = fb * 1024 + c16 * 32 + (lane & 7) * 4;
    const u32 xp = __builtin_amdgcn_raw_buffer_load_b32(rx, xo, 0, 0);
    const u32 off = WS_P2 + ((fb * 32 + c16) * 32 + (lane >> 3)) * 128 + (lane & 7) * 16;
    v4u p[4];
#pragma unroll 1
    for (u32 spin = 0; spin < SPIN_CAP; ++spin) {
      asm volatile("" ::: "memory");
#pragma unroll
      for (int i = 0; i < 4; ++i) p[i] = LDSC128(rws, off + i * 1024, 0);
      bool ok = true;
#pragma unroll
      for (int i = 0; i < 4; ++i) ok &= (p[i].y == tag) & (p[i].w == tag);
      if (__all(ok)) break;
    }
    float s0 = (__uint_as_float(p[0].x) + __uint_as_float(p[1].x)) + (__uint_as_float(p[2].x) + __uint_as_float(p[3].x));
    float s1 = (__uint_as_float(p[0].z) + __uint_as_float(p[1].z)) + (__uint_as_float(p[2].z) + __uint_as_float(p[3].z));
    s0 += dpp<ROR8>(s0); s1 += dpp<ROR8>(s1);
    s0 = x16_sum(s0); s1 = x16_sum(s1);
    if (lane < 8) ((GAS u32*)a.out)[(fb * 512 + c16 * 16) / 2 + lane] = cvtpk(bflo(xp) + s0, bfhi(xp) + s1);
    if (bid == 0 && lane == 0) __hip_atomic_store((GAS u32*)(a.ws + WS_EPOCH), tag, __ATOMIC_RELAXED, __HIP_MEMORY_SCOPE_AGENT);
  }
  STAMP(11);
}
}

extern "C" void kernel_launch(void* const* d_in, const int* in_sizes, int n_in, void* d_out, int out_size, void* d_ws, size_t ws_size, hipStream_t stream) {
  using namespace fused;
  Args a;
  a.x = (const u16*)d_in[0]; a.kc = (const u16*)d_in[1]; a.vc = (const u16*)d_in[2]; a.wq = (const u16*)d_in[3]; a.wo = (const u16*)d_in[4];
  a.w1 = (const u16*)d_in[6]; a.w2 = (const u16*)d_in[7]; a.out = (u16*)d_out; a.ws = (unsigned char*)d_ws;
  hipLaunchKernelGGL(fused_decode, dim3(256), dim3(NTHR), 0, stream, a);
}
```

```cpp
#include <hip/hip_runtime.h>
#include <hip/hip_bf16.h>

namespace fused {
typedef unsigned u32; typedef unsigned long long u64; typedef unsigned short u16;
typedef u32 v4u __attribute__((ext_vector_type(4)));
typedef u32 v2u __attribute__((ext_vector_type(2)));
typedef float v4f __attribute__((ext_vector_type(4)));
typedef float v2f __attribute__((ext_vector_type(2)));
typedef __bf16 bf8 __attribute__((ext_vector_type(8)));
typedef __bf16 bf2 __attribute__((ext_vector_type(2)));
#define LAS __attribute__((address_space(3)))
#define GAS __attribute__((address_space(1)))

constexpr int DM = 512, SK = 4096, DH = 128, WINK = 1024, FF = 1792, NQ = 4096;
constexpr int NAUX = 4, NSTR = 8, NWAVE = NAUX + 1 + NSTR, NTHR = NWAVE * 64;
constexpr int AUX_NT = 2, AUX_SC1 = 16;
constexpr u32 RSRC3 = 0x00020000u;
constexpr u32 SPIN_CAP = 1u << 16;
#define KW 6
#define QDELAY 20
#define HDELAY 20
#define FDELAY 18
#define AUX_OUT 0
#define PACE 10
#define KB 1
#define TSL 6
#define WODELAY 12
#define SDELAY 12
#define TOUCH 1
#define QT1 330
#define QT2 0
#define HT1 540
#define HGDELAY 40
#define QGAP 12

constexpr u32 WS_EPOCH = 0;
constexpr u32 WS_QG = 0x1000;
constexpr u32 WS_HQ = 0x81000;
constexpr u32 WS_HG = 0xC0000;
constexpr u32 WS_ST = 0xD0000;
constexpr u32 WS_PO = 0xE0000;
constexpr u32 WS_P2 = 0x160000;
constexpr u32 WS_END = 0x260000;
constexpr u32 WS_STAMP = 0x300000;

constexpr int L_CNT = 0;
constexpr int C_X = 0, C_P1 = 1, C_Q = 2, C_V = 3, C_QK = 4, C_TAG = 5, C_P2 = 6, C_PV = 7;
constexpr int L_MX = 64;
constexpr int L_QL = L_MX + 256, QL_STR = 320;
constexpr int L_OA = L_QL + 1280, OA_STR = 272;
constexpr int L_HA = L_OA + 8 * OA_STR;
constexpr int L_BN = L_HA + 8 * OA_STR;
constexpr int L_XN = L_BN + 2048 + 1536, XN_STR = 1040;
constexpr int L_RED1 = L_XN + 8 * XN_STR;
constexpr int L_WT = L_RED1 + 16384;
constexpr int L_K = L_WT + 32768;
constexpr int L_BYTES = L_K + 65536;
static_assert(L_XN % 16 == 0 && L_RED1 % 16 == 0 && L_WT % 16 == 0 && L_K % 16 == 0 && L_BYTES <= 163840 && L_BYTES > 81920, "LDS map");

__device__ __forceinline__ u32 cvtpk(float lo, float hi) { return __builtin_bit_cast(u32, __builtin_convertvector((v2f{lo, hi}), bf2)); }
__device__ __forceinline__ float bflo(u32 p) { return __builtin_bit_cast(float, p << 16); }
__device__ __forceinline__ float bfhi(u32 p) { return __builtin_bit_cast(float, p & 0xffff0000u); }
__device__ __forceinline__ float rbf(float x) { return bflo(cvtpk(x, x)); }
template <int CTRL> __device__ __forceinline__ float dpp(float x) {
  return __builtin_bit_cast(float, __builtin_amdgcn_mov_dpp(__builtin_bit_cast(int, x), CTRL, 0xf, 0xf, true));
}
constexpr int XOR1 = 0xB1, XOR2 = 0x4E, HMIR = 0x141, RMIR = 0x140, ROR8 = 0x128;
__device__ __forceinline__ float x32_sum(float x) {
  auto t = __builtin_amdgcn_permlane32_swap(__float_as_uint(x), __float_as_uint(x), false, false);
  return __uint_as_float(t[0]) + __uint_as_float(t[1]);
}
__device__ __forceinline__ float x32_max(float x) {
  auto t = __builtin_amdgcn_permlane32_swap(__float_as_uint(x), __float_as_uint(x), false, false);
  return fmaxf(__uint_as_float(t[0]), __uint_as_float(t[1]));
}
__device__ __forceinline__ float x16_sum(float x) {
  auto s = __builtin_amdgcn_permlane16_swap(__float_as_uint(x), __float_as_uint(x), false, false);
  return x32_sum(__uint_as_float(s[0]) + __uint_as_float(s[1]));
}
__device__ __forceinline__ float x16_max(float x) {
  auto s = __builtin_amdgcn_permlane16_swap(__float_as_uint(x), __float_as_uint(x), false, false);
  return x32_max(fmaxf(__uint_as_float(s[0]), __uint_as_float(s[1])));
}
__device__ __forceinline__ float half_sum(float x) {
  x += dpp<XOR1>(x); x += dpp<XOR2>(x); x += dpp<HMIR>(x); x += dpp<RMIR>(x);
  auto s = __builtin_amdgcn_permlane16_swap(__float_as_uint(x), __float_as_uint(x), false, false);
  return __uint_as_float(s[0]) + __uint_as_float(s[1]);
}
__device__ __forceinline__ float wave_sum(float x) {
  x += dpp<XOR1>(x); x += dpp<XOR2>(x); x += dpp<HMIR>(x); x += dpp<RMIR>(x); return x16_sum(x);
}
__device__ __forceinline__ v2u pack24(float a_, float b_, u32 tag16) {
  const u32 ua = __float_as_uint(a_) + 0x80u, ub = __float_as_uint(b_) + 0x80u;
  return v2u{__builtin_amdgcn_perm(ub, ua, 0x05030201u), __builtin_amdgcn_perm(tag16, ub, 0x05040302u)};
}
__device__ __forceinline__ float un24a(u32 d0) { return __uint_as_float(d0 << 8); }
__device__ __forceinline__ float un24b(u32 d0, u32 d1) { return __uint_as_float(__builtin_amdgcn_perm(d1, d0, 0x0504030Cu)); }
__device__ __forceinline__ v4f mfma32(v4u a, v4u b, v4f c) {
  return __builtin_amdgcn_mfma_f32_16x16x32_bf16(__builtin_bit_cast(bf8, a), __builtin_bit_cast(bf8, b), c, 0, 0, 0);
}
__device__ __forceinline__ v2u trread(LAS unsigned char* p) {
  typedef short v4s __attribute__((ext_vector_type(4)));
  return __builtin_bit_cast(v2u, __builtin_amdgcn_ds_read_tr16_b64_v4i16((LAS v4s*)p));
}
#define BAR() do { asm volatile("s_waitcnt lgkmcnt(0)" ::: "memory"); __builtin_amdgcn_s_barrier(); asm volatile("" ::: "memory"); } while (0)
#define SB() __builtin_amdgcn_sched_barrier(0)
#define LGKM0() asm volatile("s_waitcnt lgkmcnt(0)" ::: "memory")
#define PIN(x) asm volatile("" : "+v"(x))
__device__ __forceinline__ __amdgpu_buffer_rsrc_t mkrs(const void* p, u32 bytes) { return __builtin_amdgcn_make_buffer_rsrc((void*)p, 0, (int)bytes, RSRC3); }
#define LDNT128(r, vo, so) __builtin_amdgcn_raw_buffer_load_b128((r), (vo), (so), AUX_NT)
#define AUX_W 2
#define LDW128(r, vo, so) __builtin_amdgcn_raw_buffer_load_b128((r), (vo), (so), AUX_W)
#define LDSC128(r, vo, so) __builtin_amdgcn_raw_buffer_load_b128((r), (vo), (so), AUX_SC1)
#define STSC128(v, r, vo, so) __builtin_amdgcn_raw_buffer_store_b128((v), (r), (vo), (so), AUX_SC1)
__device__ __forceinline__ u32 cnt_add(LAS unsigned char* L, int c) {
  asm volatile("" ::: "memory");
  return __hip_atomic_fetch_add((LAS u32*)(L + L_CNT) + c, 1u, __ATOMIC_RELAXED, __HIP_MEMORY_SCOPE_WORKGROUP);
}
__device__ __forceinline__ void cnt_wait(LAS unsigned char* L, int c, u32 n) {
#pragma unroll 1
  for (u32 spin = 0; spin < (1u << 22); ++spin) {
    if (__hip_atomic_load((LAS u32*)(L + L_CNT) + c, __ATOMIC_RELAXED, __HIP_MEMORY_SCOPE_WORKGROUP) >= n) break;
    __builtin_amdgcn_s_sleep(1);
  }
  asm volatile("" ::: "memory");
}

#define STAMP(i) do { } while (0)

struct Args { const u16* x; const u16* kc; const u16* vc; const u16* wq; const u16* wo; const u16* w1; const u16* w2; u16* out; unsigned char* ws; };

__global__ void __launch_bounds__(NTHR, 4) fused_decode(Args a) {
  __shared__ __attribute__((aligned(16))) unsigned char smem[L_BYTES];
  LAS unsigned char* L = (LAS unsigned char*)smem;
  const int tid = threadIdx.x, lane = tid & 63, wv = __builtin_amdgcn_readfirstlane(tid >> 6);
  if (tid < 8) ((LAS u32*)(L + L_CNT))[tid] = 0u;
  const int bid = blockIdx.x, hkv = bid & 7, j = bid >> 3;
  const int g = lane >> 4, c = lane & 15;
  asm volatile("" :: "s"(a.x), "s"(a.kc), "s"(a.vc), "s"(a.wq), "s"(a.wo), "s"(a.w1), "s"(a.w2), "s"(a.out), "s"(a.ws));
  BAR();
  STAMP(0);
  const int b = j >> 2, s = j & 3;

  const __amdgpu_buffer_rsrc_t rx = mkrs(a.x, 8192);
  if (wv > NAUX) {
    const int w = wv - NAUX - 1;
    const size_t kvbase = ((size_t)(b * 8 + hkv) * SK + (SK - WINK) + s * 256 + w * 32) * DH * 2;
    const __amdgpu_buffer_rsrc_t rk = mkrs((const unsigned char*)a.kc + kvbase, 8192);
    const __amdgpu_buffer_rsrc_t rv = mkrs((const unsigned char*)a.vc + kvbase, 8192);
    v4u K[KW], V[8];
    const u32 vvo = g * 1024 + c * 16;
    const u32 kvo = g * 256 + c * 16;
    LAS unsigned char* kl = L + L_K + w * 8192;
#define KLD(u) K[(u) % KW] = LDNT128(rk, kvo + ((u) & 3) * 1024, ((u) >> 2) * 4096)
#define KST(u) *(LAS v4u*)(kl + (4 * (u) + g) * 256 + ((c ^ ((4 * (u) + g) & 15)) * 16)) = K[(u) % KW]
#define VLD(i) V[i] = LDNT128(rv, vvo + ((i) & 3) * 256, ((i) >> 2) * 4096)
    SB(); __builtin_amdgcn_s_barrier(); SB();
    if (SDELAY > 0) __builtin_amdgcn_s_sleep(SDELAY);
#pragma unroll
    for (int n = 0; n < 16; ++n) {
      if (n >= KB && PACE > 0) __builtin_amdgcn_s_sleep(PACE);
      if (n >= KW) { if (n - KW < 8) { KST(n - KW); } else { PIN(V[n - KW - 8]); } }
      SB();
      if (n < 8) { KLD(n); } else { VLD(n - 8); }
      SB();
      if (n == 7 + KW) STAMP(2);
    }
    if (lane == 0) cnt_add(L, C_V);
#pragma unroll
    for (int n = 16 - KW; n < 8; ++n) { KST(n); }
    STAMP(3);
    v4u kf[8];
#pragma unroll
    for (int i = 0; i < 4; ++i) { kf[i] = *(LAS v4u*)(kl + c * 256 + (((4 * i + g) ^ c) * 16)); kf[4 + i] = *(LAS v4u*)(kl + 4096 + c * 256 + (((4 * i + g) ^ c) * 16)); }
#pragma unroll
    for (int i = 0; i < 8; ++i) PIN(kf[i]);
    cnt_wait(L, C_Q, 1);
    STAMP(4);
    float sc[8];
    {
      v4u qf[4];
#pragma unroll
      for (int i = 0; i < 4; ++i) qf[i] = *(LAS v4u*)(L + L_QL + (c >> 2) * QL_STR + i * 64 + g * 16);
      v4f s0 = {0.f, 0.f, 0.f, 0.f}, s1 = s0;
#pragma unroll
      for (int i = 0; i < 4; ++i) { s0 = mfma32(kf[i], qf[i], s0); s1 = mfma32(kf[4 + i], qf[i], s1); }
      constexpr float SCALE = 0.08838834764831845f;
#pragma unroll
      for (int r = 0; r < 4; r += 2) {
        const u32 p0 = cvtpk(s0[r], s0[r + 1]), p1 = cvtpk(s1[r], s1[r + 1]);
        const u32 t0 = cvtpk(bflo(p0) * SCALE, bfhi(p0) * SCALE), t1 = cvtpk(bflo(p1) * SCALE, bfhi(p1) * SCALE);
        sc[r] = bflo(t0); sc[r + 1] = bfhi(t0); sc[4 + r] = bflo(t1); sc[4 + r + 1] = bfhi(t1);
      }
    }
    float mx = fmaxf(fmaxf(fmaxf(sc[0], sc[1]), fmaxf(sc[2], sc[3])), fmaxf(fmaxf(sc[4], sc[5]), fmaxf(sc[6], sc[7])));
    mx = x16_max(mx);
    v4u P;
    {
      constexpr float L2E = 1.4426950408889634f;
      float e[8], ls;
      const float mxl2 = -mx * L2E;
#pragma unroll
      for (int i = 0; i < 8; ++i) e[i] = __builtin_amdgcn_exp2f(fmaf(sc[i], L2E, mxl2));
      ls = ((e[0] + e[1]) + (e[2] + e[3])) + ((e[4] + e[5]) + (e[6] + e[7]));
      P = v4u{cvtpk(e[0], e[1]), cvtpk(e[2], e[3]), cvtpk(e[4], e[5]), cvtpk(e[6], e[7])};
      ls = x16_sum(ls);
      LAS float* mxl = (LAS float*)(L + L_MX);
      if (g == 0 && (c & 3) == 0) { mxl[(c >> 2) * 8 + w] = mx; mxl[32 + (c >> 2) * 8 + w] = ls; }
      if (lane == 0) cnt_add(L, C_QK);
    }
    STAMP(5);
    {
      v4f o[8];
#pragma unroll
      for (int jj = 0; jj < 8; ++jj) {
        const u32 sel = (jj & 1) ? 0x07060302u : 0x05040100u;
        v4u bf;
#pragma unroll
        for (int pr = 0; pr < 4; ++pr) bf[pr] = __builtin_amdgcn_perm(V[2 * pr + 1][jj >> 1], V[2 * pr][jj >> 1], sel);
        o[jj] = mfma32(P, bf, v4f{0.f, 0.f, 0.f, 0.f});
      }
      LAS unsigned char* r2 = L + L_WT + w * 2048 + g * 512 + c * 32;
      *(LAS v4f*)r2 = v4f{o[0][0], o[1][0], o[2][0], o[3][0]};
      *(LAS v4f*)(r2 + 16) = v4f{o[4][0], o[5][0], o[6][0], o[7][0]};
      if (lane == 0) cnt_add(L, C_PV);
    }
    STAMP(6);
    LGKM0();
    return;
  }

  const int hh = 4 * hkv + (j & 3), cs = j >> 2;
  const int ks = 2 * hkv + (j & 3), n1 = 16 * hkv + 8 * ((j & 3) - 2) + cs, t1 = n1 >> 2, kq1 = n1 & 3;
  const bool ffn2 = (j & 3) < 2 && ks < 14, ffn1 = (j & 3) >= 2 && n1 < 112;
  const __amdgpu_buffer_rsrc_t rws = mkrs(a.ws, WS_END);
  __builtin_amdgcn_s_setprio(2);
  if (wv == NAUX) {
    const u64 tstart = __builtin_amdgcn_s_memrealtime();
    const u32 ep = __builtin_amdgcn_raw_buffer_load_b32(rws, WS_EPOCH, 0, AUX_SC1);
    u32 tch;
    {
      const unsigned char* tp = (const unsigned char*)a.ws;
      size_t to = 0;
      const int t = lane & 31;
      if (t < 8) { tp = (const unsigned char*)a.w1; to = (size_t)(t * 73) * (FF * 2); }
      else if (t < 10) { tp = (const unsigned char*)a.wo; to = (size_t)(hh * 128 + (t - 8) * 127) * (DM * 2); }
      else if (t < 12) { tp = (const unsigned char*)a.w2; to = (size_t)((ffn2 ? ks : 0) * 128 + (t - 10) * 127) * (DM * 2); }
      else if (t < 14) { tp = (const unsigned char*)a.vc; to = ((size_t)(b * 8 + hkv) * SK + (SK - WINK) + s * 256 + (t - 12) * 255) * DH * 2; }
      else if (t < 15) { tp = (const unsigned char*)a.out; to = 0; }
      else if (t < 24) { to = (size_t)(t - 15) * 0x38000; }
      tch = *(const GAS u32*)(tp + to);
    }
    SB(); __builtin_amdgcn_s_barrier(); SB();
    const u32 tag = __builtin_amdgcn_readfirstlane(ep) + 1u;
    if (lane == 0) ((LAS u32*)(L + L_CNT))[C_TAG] = tag;
    asm volatile("" :: "v"(tch));
    const u32 tg16 = tag & 0xffffu;
    const int kq = j & 3;
    cnt_wait(L, C_P1, NAUX);
    STAMP(2);
#pragma unroll
    for (int tl = 0; tl < 2; ++tl) {
      if (tl == 1) { if (!ffn1) break; cnt_wait(L, C_P2, NAUX); }
#pragma unroll
      for (int s2 = 0; s2 < 2; ++s2) {
        const int pb = 4 * s2 + (lane >> 4);
        LAS v4f* red = (LAS v4f*)(L + L_RED1 + tl * 8192 + ((lane & 15) >> 2) * 512 + (lane & 3) * 128 + pb * 16);
        const v4f sv = (red[0] + red[128]) + (red[256] + red[384]);
        const v2u g0 = pack24(sv.x, sv.y, tg16), g1 = pack24(sv.z, sv.w, tg16);
        const u32 dst = tl == 0 ? WS_QG + (((pb * 8 + hkv) * 4 + kq) * 8 + cs) * 256 : WS_HQ + ((pb * 28 + t1) * 4 + kq1) * 256;
        STSC128((v4u{g0.x, g0.y, g1.x, g1.y}), rws, dst + (lane & 15) * 16, 0);
      }
    }
    STAMP(3);
#define T16OK(x) ((((x).y >> 16) == tg16) & (((x).w >> 16) == tg16))
    {
      const u32 off = WS_QG + (b * 8 + hkv) * 8192 + lane * 16;
      const u32 hoff = WS_HQ + (((2 * kq1 + ((lane >> 4) & 1)) * 28 + (ffn1 ? t1 : 0)) * 4 + 2 * (lane >> 5)) * 256 + (lane & 15) * 16;
      v4u qp[8], qr[8], h0 = {0u, 0u, 0u, 0u}, h1 = h0;
      while (__builtin_amdgcn_s_memrealtime() < tstart + QT1) __builtin_amdgcn_s_sleep(TSL);
      asm volatile("" ::: "memory");
#pragma unroll
      for (int i = 0; i < 8; ++i) qp[i] = LDSC128(rws, off + (i >> 1) * 2048 + (i & 1) * 1024, 0);
      if (ffn1) { h0 = LDSC128(rws, hoff, 0); h1 = LDSC128(rws, hoff + 256, 0); }
      SB();
      if (QT2 > 0) {
        while (__builtin_amdgcn_s_memrealtime() < tstart + QT2) __builtin_amdgcn_s_sleep(TSL);
        asm volatile("" ::: "memory");
#pragma unroll
        for (int i = 0; i < 8; ++i) qr[i] = LDSC128(rws, off + (i >> 1) * 2048 + (i & 1) * 1024, 0);
        SB();
      }
      bool qok = true;
#pragma unroll
      for (int i = 0; i < 8; ++i) qok &= T16OK(qp[i]);
      if (!__all(qok)) {
        if (QT2 > 0) {
#pragma unroll
          for (int i = 0; i < 8; ++i) qp[i] = qr[i];
        }
#pragma unroll 1
        for (u32 spin = 0; spin < SPIN_CAP; ++spin) {
          bool ok = true;
#pragma unroll
          for (int i = 0; i < 8; ++i) ok &= T16OK(qp[i]);
          if (__all(ok)) break;
          asm volatile("" ::: "memory");
#pragma unroll
          for (int i = 0; i < 8; ++i) qp[i] = LDSC128(rws, off + (i >> 1) * 2048 + (i & 1) * 1024, 0);
        }
      }
#pragma unroll
      for (int hf = 0; hf < 2; ++hf) {
        float v0 = 0.f, v1 = 0.f, v2 = 0.f, v3 = 0.f;
#pragma unroll
        for (int k4 = 0; k4 < 4; ++k4) { const v4u p = qp[2 * k4 + hf]; v0 += un24a(p.x); v1 += un24b(p.x, p.y); v2 += un24a(p.z); v3 += un24b(p.z, p.w); }
        *(LAS v2u*)(L + L_QL + (2 * hf + (lane >> 5)) * QL_STR + (lane & 31) * 8) = v2u{cvtpk(v0, v1), cvtpk(v2, v3)};
      }
      if (lane == 0) cnt_add(L, C_Q);
      STAMP(4);
      if (ffn1) {
#pragma unroll 1
        for (u32 spin = 0; spin < SPIN_CAP; ++spin) {
          if (__all(T16OK(h0) & T16OK(h1))) break;
          asm volatile("" ::: "memory");
          h0 = LDSC128(rws, hoff, 0); h1 = LDSC128(rws, hoff + 256, 0);
        }
        float hv[4] = {x32_sum(un24a(h0.x) + un24a(h1.x)), x32_sum(un24b(h0.x, h0.y) + un24b(h1.x, h1.y)),
                       x32_sum(un24a(h0.z) + un24a(h1.z)), x32_sum(un24b(h0.z, h0.w) + un24b(h1.z, h1.w))};
#pragma unroll
        for (int r = 0; r < 4; ++r) { const float hb = rbf(hv[r]); hv[r] = hb * __builtin_amdgcn_rcpf(1.0f + __builtin_amdgcn_exp2f(-1.4426950408889634f * hb)); }
        if (lane < 32) STSC128((v4u{cvtpk(hv[0], hv[1]), tag, cvtpk(hv[2], hv[3]), tag}), rws, WS_HG + (t1 * 8 + 2 * kq1 + (lane >> 4)) * 256 + (lane & 15) * 16, 0);
      }
    }
    if (ffn2) {
      while (__builtin_amdgcn_s_memrealtime() < tstart + HT1) __builtin_amdgcn_s_sleep(TSL);
      const u32 off = WS_HG + ks * 4096 + lane * 16;
      v4u hv[4];
#pragma unroll 1
      for (u32 spin = 0; spin < SPIN_CAP; ++spin) {
        asm volatile("" ::: "memory");
        bool ok = true;
#pragma unroll
        for (int i = 0; i < 4; ++i) hv[i] = LDSC128(rws, off + i * 1024, 0);
#pragma unroll
        for (int i = 0; i < 4; ++i) ok &= (hv[i].y == tag) & (hv[i].w == tag);
        if (__all(ok)) break;
        __builtin_amdgcn_s_sleep(16);
      }
#pragma unroll
      for (int i = 0; i < 4; ++i)
        *(LAS v2u*)(L + L_HA + (4 * (i & 1) + (lane >> 4)) * OA_STR + (64 * (i >> 1) + 4 * (lane & 15)) * 2) = v2u{hv[i].x, hv[i].z};
    }
    STAMP(5);
    LGKM0();
    return;
  }

  const int w = wv;
  v4u xr[2], wr[4], w1r[4];
  {
    const u32 xo = (2 * w + (lane >> 5)) * 1024 + (lane & 31) * 16;
    xr[0] = __builtin_amdgcn_raw_buffer_load_b128(rx, xo, 0, 0);
    xr[1] = __builtin_amdgcn_raw_buffer_load_b128(rx, xo + 512, 0, 0);
    const __amdgpu_buffer_rsrc_t rwq = mkrs(a.wq, DM * NQ * 2);
    const u32 vo = (128 * (j & 3) + 32 * w + (lane >> 3)) * (NQ * 2) + (hkv * 512 + cs * 64) * 2 + (lane & 7) * 16;
#pragma unroll
    for (int u = 0; u < 4; ++u) wr[u] = LDNT128(rwq, vo, u * 8 * NQ * 2);
    const __amdgpu_buffer_rsrc_t rw1 = mkrs(a.w1, ffn1 ? DM * FF * 2 : 0);
    const u32 vo1 = (128 * kq1 + 32 * w + (lane >> 3)) * (FF * 2) + (ffn1 ? t1 : 0) * 128 + (lane & 7) * 16;
#pragma unroll
    for (int u = 0; u < 4; ++u) w1r[u] = LDNT128(rw1, vo1, u * 8 * FF * 2);
  }
  SB(); __builtin_amdgcn_s_barrier(); SB();
  {
    float xv[16];
#pragma unroll
    for (int h2 = 0; h2 < 2; ++h2)
#pragma unroll
      for (int i = 0; i < 4; ++i) { xv[8 * h2 + 2 * i] = bflo(xr[h2][i]); xv[8 * h2 + 2 * i + 1] = bfhi(xr[h2][i]); }
    float sm = (((xv[0] + xv[1]) + (xv[2] + xv[3])) + ((xv[4] + xv[5]) + (xv[6] + xv[7]))) + (((xv[8] + xv[9]) + (xv[10] + xv[11])) + ((xv[12] + xv[13]) + (xv[14] + xv[15])));
    sm = half_sum(sm);
    const float mean = sm * (1.0f / DM);
    float s0 = 0.f, s1 = 0.f;
#pragma unroll
    for (int i = 0; i < 8; ++i) { const float d0 = xv[i] - mean, d1 = xv[8 + i] - mean; s0 = fmaf(d0, d0, s0); s1 = fmaf(d1, d1, s1); }
    const float ss = half_sum(s0 + s1);
    const float mb = rbf(mean), var = rbf(ss * (1.0f / DM));
    const float den = rbf(__builtin_amdgcn_sqrtf(rbf(var + 1e-5f)));
    const float inv = __builtin_amdgcn_rcpf(den);
#pragma unroll
    for (int h2 = 0; h2 < 2; ++h2) {
      v4u o;
#pragma unroll
      for (int i = 0; i < 4; ++i) {
        const u32 dd = cvtpk(xv[8 * h2 + 2 * i] - mb, xv[8 * h2 + 2 * i + 1] - mb);
        o[i] = cvtpk(bflo(dd) * inv, bfhi(dd) * inv);
      }
      *(LAS v4u*)(L + L_XN + (2 * w + (lane >> 5)) * XN_STR + h2 * 512 + (lane & 31) * 16) = o;
    }
    if (lane == 0) cnt_add(L, C_X);
  }
  STAMP(1);
  {
    LAS unsigned char* wt = L + L_WT + w * 8192;
    const int wst = (lane >> 3) * 128 + (((lane & 7) ^ ((lane >> 3) & 6)) * 16);
    const int wf = (2 * g + (c >> 3)) & 3;
    const int wrd = (4 * g + (c >> 2)) * 128 + (c & 1) * 8;
#pragma unroll
    for (int u = 0; u < 4; ++u) *(LAS v4u*)(wt + u * 1024 + wst) = wr[u];
    asm volatile("" ::: "memory");
    v4u wa1[4];
#pragma unroll
    for (int ct = 0; ct < 4; ++ct) {
      LAS unsigned char* tp = wt + wrd + (((2 * ct + ((c & 3) >> 1)) ^ (2 * wf)) * 16);
      const v2u p0 = trread(tp), p1 = trread(tp + 2048);
      wa1[ct] = v4u{p0.x, p0.y, p1.x, p1.y};
    }
    PIN(wa1[0]); PIN(wa1[1]); PIN(wa1[2]); PIN(wa1[3]);
    cnt_wait(L, C_X, NAUX);
    LAS unsigned char* xp = L + L_XN + (c & 7) * XN_STR + (32 * w + 4 * g) * 2;
    {
      const v2u lo = *(LAS v2u*)(xp + 256 * (j & 3)), hi = *(LAS v2u*)(xp + 256 * (j & 3) + 32);
      const v4u xb = v4u{lo.x, lo.y, hi.x, hi.y};
#pragma unroll
      for (int ct = 0; ct < 4; ++ct) {
        const v4f d = mfma32(wa1[ct], xb, v4f{0.f, 0.f, 0.f, 0.f});
        if (c < 8) *(LAS v4f*)(L + L_RED1 + w * 2048 + ct * 512 + g * 128 + c * 16) = d;
      }
    }
    if (lane == 0) cnt_add(L, C_P1);
    STAMP(2);
    if (ffn1) {
#pragma unroll
      for (int u = 0; u < 4; ++u) *(LAS v4u*)(wt + 4096 + u * 1024 + wst) = w1r[u];
      asm volatile("" ::: "memory");
      const v2u lo = *(LAS v2u*)(xp + 256 * kq1), hi = *(LAS v2u*)(xp + 256 * kq1 + 32);
      const v4u xb = v4u{lo.x, lo.y, hi.x, hi.y};
#pragma unroll
      for (int ct = 0; ct < 4; ++ct) {
        LAS unsigned char* tp = wt + 4096 + wrd + (((2 * ct + ((c & 3) >> 1)) ^ (2 * wf)) * 16);
        const v2u p0 = trread(tp), p1 = trread(tp + 2048);
        const v4f d = mfma32(v4u{p0.x, p0.y, p1.x, p1.y}, xb, v4f{0.f, 0.f, 0.f, 0.f});
        if (c < 8) *(LAS v4f*)(L + L_RED1 + 8192 + w * 2048 + ct * 512 + g * 128 + c * 16) = d;
      }
      if (lane == 0) cnt_add(L, C_P2);
    }
  }
  STAMP(3);
  cnt_wait(L, C_V, NSTR);
  STAMP(4);
  if (WODELAY > 0) __builtin_amdgcn_s_sleep(WODELAY);
  v4u wor[4], w2r[4];
  {
    const __amdgpu_buffer_rsrc_t rwo = mkrs(a.wo, NQ * DM * 2);
    const __amdgpu_buffer_rsrc_t rw2 = mkrs(a.w2, ffn2 ? FF * DM * 2 : 0);
    const u32 vo = (hh * 128 + 32 * w + (lane >> 3)) * (DM * 2) + cs * 128 + (lane & 7) * 16;
    const u32 vo2 = ((ffn2 ? ks : 0) * 128 + 32 * w + (lane >> 3)) * (DM * 2) + cs * 128 + (lane & 7) * 16;
#pragma unroll
    for (int u = 0; u < 4; ++u) wor[u] = LDNT128(rwo, vo, u * 8 * DM * 2);
#pragma unroll
    for (int u = 0; u < 4; ++u) w2r[u] = LDNT128(rw2, vo2, u * 8 * DM * 2);
  }
  SB();
  STAMP(5);
  cnt_wait(L, C_QK, NSTR);
  const u32 tag = ((LAS u32*)(L + L_CNT))[C_TAG];
  v4f fw;
  {
    LAS float* mxl = (LAS float*)(L + L_MX);
    const v4f m0 = *(LAS v4f*)(mxl + w * 8), m1 = *(LAS v4f*)(mxl + w * 8 + 4);
    const v4f l0 = *(LAS v4f*)(mxl + 32 + w * 8), l1 = *(LAS v4f*)(mxl + 32 + w * 8 + 4);
    const float M = fmaxf(fmaxf(fmaxf(m0.x, m0.y), fmaxf(m0.z, m0.w)), fmaxf(fmaxf(m1.x, m1.y), fmaxf(m1.z, m1.w)));
    constexpr float L2E = 1.4426950408889634f;
    v4f f0, f1;
#pragma unroll
    for (int i = 0; i < 4; ++i) { f0[i] = __builtin_amdgcn_exp2f((m0[i] - M) * L2E); f1[i] = __builtin_amdgcn_exp2f((m1[i] - M) * L2E); }
    float Ls = 0.f;
#pragma unroll
    for (int i = 0; i < 4; ++i) { Ls = fmaf(f0[i], l0[i], Ls); Ls = fmaf(f1[i], l1[i], Ls); }
    const float inv = __builtin_amdgcn_rcpf(Ls);
    const bool lo = lane < 32;
    fw = v4f{lo ? f0.x : f1.x, lo ? f0.y : f1.y, lo ? f0.z : f1.z, lo ? f0.w : f1.w} * inv;
    if (w == 0) {
      const int hd = lane & 3;
      const v4f a0 = *(LAS v4f*)(mxl + hd * 8), a1 = *(LAS v4f*)(mxl + hd * 8 + 4);
      const v4f b0 = *(LAS v4f*)(mxl + 32 + hd * 8), b1 = *(LAS v4f*)(mxl + 32 + hd * 8 + 4);
      const float Mh = fmaxf(fmaxf(fmaxf(a0.x, a0.y), fmaxf(a0.z, a0.w)), fmaxf(fmaxf(a1.x, a1.y), fmaxf(a1.z, a1.w)));
      float Lh = 0.f;
#pragma unroll
      for (int i = 0; i < 4; ++i) { Lh = fmaf(__builtin_amdgcn_exp2f((a0[i] - Mh) * L2E), b0[i], Lh); Lh = fmaf(__builtin_amdgcn_exp2f((a1[i] - Mh) * L2E), b1[i], Lh); }
      if (lane < 4) STSC128((v4u{__float_as_uint(Mh), tag, __float_as_uint(Lh), tag}), rws, WS_ST + ((hkv * 8 + b) * 4 + s) * 128 + lane * 16, 0);
    }
  }
  STAMP(6);
  cnt_wait(L, C_PV, NSTR);
  STAMP(7);
  {
    LAS v4f* r2 = (LAS v4f*)(L + L_WT + (lane >> 5) * 8192 + w * 512 + (lane & 31) * 16);
    v4f o = r2[0] * fw[0];
    o += r2[128] * fw[1]; o += r2[256] * fw[2]; o += r2[384] * fw[3];
    o.x = x32_sum(o.x); o.y = x32_sum(o.y); o.z = x32_sum(o.z); o.w = x32_sum(o.w);
    if (lane < 32) STSC128((v4u{cvtpk(o.x, o.y), tag, cvtpk(o.z, o.w), tag}), rws, WS_PO + (((4 * hkv + w) * 8 + b) * 4 + s) * 512 + lane * 16, 0);
  }
  STAMP(8);
  const u32 soff = WS_ST + ((hkv * 8 + 2 * w) * 4 + (lane >> 5)) * 128 + (hh & 3) * 16;
  v4u st[4];
#pragma unroll
  for (int i = 0; i < 4; ++i) st[i] = LDSC128(rws, soff + i * 256, 0);
  const u32 xres = __builtin_amdgcn_raw_buffer_load_b32(rx, (bid & 7) * 1024 + (bid >> 3) * 32 + (lane & 7) * 4, 0, 0);
  SB();
  const u32 poff = WS_PO + ((hh * 8 + 2 * w) * 4) * 512 + lane * 16;
  v4u po[4];
  __builtin_amdgcn_s_sleep(HDELAY);
#pragma unroll
  for (int i = 0; i < 4; ++i) po[i] = LDSC128(rws, poff + i * 1024, 0);
  SB();
  LAS unsigned char* wl = L + L_K;
#pragma unroll
  for (int u = 0; u < 4; ++u) {
    *(LAS v4u*)(wl + (32 * w + 8 * u + (lane >> 3)) * 160 + (lane & 7) * 16) = wor[u];
    *(LAS v4u*)(wl + 20480 + (32 * w + 8 * u + (lane >> 3)) * 160 + (lane & 7) * 16) = w2r[u];
  }
  BAR();
  v4u wa[4];
  v4f d1 = {0.f, 0.f, 0.f, 0.f};
#pragma unroll
  for (int t = 0; t < 4; ++t) {
    LAS unsigned char* tp = wl + (32 * t + 4 * g + (c >> 2)) * 160 + w * 32 + (c & 3) * 8;
    const v2u t0 = trread(tp), t1_ = trread(tp + 16 * 160);
    wa[t] = v4u{t0.x, t0.y, t1_.x, t1_.y};
    if (ffn2) {
      const v2u t2 = trread(tp + 20480), t3 = trread(tp + 20480 + 16 * 160);
      LAS unsigned char* hp = L + L_HA + (c & 7) * OA_STR + (32 * t + 4 * g) * 2;
      const v2u hl = *(LAS v2u*)hp, hu = *(LAS v2u*)(hp + 32);
      d1 = mfma32(v4u{t2.x, t2.y, t3.x, t3.y}, v4u{hl.x, hl.y, hu.x, hu.y}, d1);
    }
  }
#pragma unroll 1
  for (u32 spin = 0; spin < SPIN_CAP; ++spin) {
    bool ok = true;
#pragma unroll
    for (int i = 0; i < 4; ++i) ok &= (st[i].y == tag) & (st[i].w == tag);
    if (__all(ok)) break;
    asm volatile("" ::: "memory");
#pragma unroll
    for (int i = 0; i < 4; ++i) st[i] = LDSC128(rws, soff + i * 256, 0);
  }
  float f0[2], f1[2];
#pragma unroll
  for (int bb = 0; bb < 2; ++bb) {
    const v4u s0 = st[2 * bb], s1 = st[2 * bb + 1];
    const float Mg = x32_max(fmaxf(__uint_as_float(s0.x), __uint_as_float(s1.x)));
    const float w0 = __builtin_amdgcn_exp2f((__uint_as_float(s0.x) - Mg) * 1.4426950408889634f) * __uint_as_float(s0.z);
    const float w1 = __builtin_amdgcn_exp2f((__uint_as_float(s1.x) - Mg) * 1.4426950408889634f) * __uint_as_float(s1.z);
    const float iw = __builtin_amdgcn_rcpf(x32_sum(w0 + w1));
    f0[bb] = w0 * iw; f1[bb] = w1 * iw;
  }
  PIN(f0[0]); PIN(f0[1]); PIN(f1[0]); PIN(f1[1]); PIN(d1);
#pragma unroll 1
  for (u32 spin = 0; spin < SPIN_CAP; ++spin) {
    bool ok = true;
#pragma unroll
    for (int i = 0; i < 4; ++i) ok &= (po[i].y == tag) & (po[i].w == tag);
    if (__all(ok)) break;
    asm volatile("" ::: "memory");
#pragma unroll
    for (int i = 0; i < 4; ++i) po[i] = LDSC128(rws, poff + i * 1024, 0);
  }
  STAMP(9);
#pragma unroll
  for (int bb = 0; bb < 2; ++bb) {
    const v4u p0 = po[2 * bb], p1 = po[2 * bb + 1];
    const float o0 = x32_sum(fmaf(f0[bb], bflo(p0.x), f1[bb] * bflo(p1.x))), o1 = x32_sum(fmaf(f0[bb], bfhi(p0.x), f1[bb] * bfhi(p1.x)));
    const float o2 = x32_sum(fmaf(f0[bb], bflo(p0.z), f1[bb] * bflo(p1.z))), o3 = x32_sum(fmaf(f0[bb], bfhi(p0.z), f1[bb] * bfhi(p1.z)));
    if (lane < 32) *(LAS v2u*)(L + L_OA + (2 * w + bb) * OA_STR + lane * 8) = v2u{cvtpk(o0, o1), cvtpk(o2, o3)};
  }
  BAR();
  {
    v4f d0 = d1, d2 = {0.f, 0.f, 0.f, 0.f};
#pragma unroll
    for (int t = 0; t < 4; ++t) {
      LAS unsigned char* op = L + L_OA + (c & 7) * OA_STR + (32 * t + 4 * g) * 2;
      const v2u lo = *(LAS v2u*)op, hi = *(LAS v2u*)(op + 32);
      if (t & 1) d2 = mfma32(wa[t], v4u{lo.x, lo.y, hi.x, hi.y}, d2);
      else d0 = mfma32(wa[t], v4u{lo.x, lo.y, hi.x, hi.y}, d0);
    }
    d0 += d2;
    LAS unsigned char* bn = L + L_BN + w * 512;
    if (c < 8) *(LAS v4f*)(bn + c * 64 + g * 16) = d0;
    asm volatile("" ::: "memory");
    const v2f pv = *(LAS v2f*)(bn + lane * 8);
    STSC128((v4u{__float_as_uint(pv.x), tag, __float_as_uint(pv.y), tag}), rws,
            WS_P2 + ((((lane >> 3) * 32 + cs * 4 + w) * 32 + hh) * 16 + (lane & 7) * 2) * 8, 0);
  }
  STAMP(10);
  if (w == 0) {
    const int fb = bid & 7, c16 = bid >> 3;
    const u32 xp = xres;
    const u32 off = WS_P2 + ((fb * 32 + c16) * 32 + (lane >> 3)) * 128 + (lane & 7) * 16;
    v4u p[4];
    __builtin_amdgcn_s_sleep(FDELAY);
#pragma unroll 1
    for (u32 spin = 0; spin < SPIN_CAP; ++spin) {
      asm volatile("" ::: "memory");
#pragma unroll
      for (int i = 0; i < 4; ++i) p[i] = LDSC128(rws, off + i * 1024, 0);
      bool ok = true;
#pragma unroll
      for (int i = 0; i < 4; ++i) ok &= (p[i].y == tag) & (p[i].w == tag);
      if (__all(ok)) break;
    }
    float s0 = (__uint_as_float(p[0].x) + __uint_as_float(p[1].x)) + (__uint_as_float(p[2].x) + __uint_as_float(p[3].x));
    float s1 = (__uint_as_float(p[0].z) + __uint_as_float(p[1].z)) + (__uint_as_float(p[2].z) + __uint_as_float(p[3].z));
    s0 += dpp<ROR8>(s0); s1 += dpp<ROR8>(s1);
    s0 = x16_sum(s0); s1 = x16_sum(s1);
    if (lane < 8) __builtin_amdgcn_raw_buffer_store_b32(cvtpk(bflo(xp) + s0, bfhi(xp) + s1), mkrs(a.out, 8192), fb * 1024 + c16 * 32 + lane * 4, 0, AUX_OUT);
    if (bid == 0 && lane == 0) __hip_atomic_store((GAS u32*)(a.ws + WS_EPOCH), tag, __ATOMIC_RELAXED, __HIP_MEMORY_SCOPE_AGENT);
  }
  STAMP(11);
}
}

extern "C" void kernel_launch(void* const* d_in, const int* in_sizes, int n_in, void* d_out, int out_size, void* d_ws, size_t ws_size, hipStream_t stream) {
  using namespace fused;
  Args a;
  a.x = (const u16*)d_in[0]; a.kc = (const u16*)d_in[1]; a.vc = (const u16*)d_in[2]; a.wq = (const u16*)d_in[3]; a.wo = (const u16*)d_in[4];
  a.w1 = (const u16*)d_in[6]; a.w2 = (const u16*)d_in[7]; a.out = (u16*)d_out; a.ws = (unsigned char*)d_ws;
  hipLaunchKernelGGL(fused_decode, dim3(256), dim3(NTHR), 0, stream, a);
}
```
